```python
import torch

def _softmax(x, axis=-1):
    return torch.softmax(x, dim=axis)
def _gelu(x):
    return torch.nn.functional.gelu(x, approximate="tanh")
def _silu(x):
    return torch.nn.functional.silu(x)
def _sigmoid(x):
    return torch.sigmoid(x)
def _layernorm(x, axis=-1, eps=1e-5):
    m = x.mean(dim=axis, keepdim=True)
    v = x.var(dim=axis, keepdim=True, unbiased=False)
    return (x - m) / torch.sqrt(v + eps)
def _scatter_add(tgt, idx, src, axis=0):
    return tgt.clone().index_add_(axis, idx, src)
def _segment_sum(src, seg, n):
    out = torch.zeros((n,) + src.shape[1:], dtype=src.dtype)
    return out.index_add_(0, seg, src)
def _pad(x, pw):
    flat = tuple(v for pair in reversed(pw) for v in pair)
    return torch.nn.functional.pad(x, flat)

kernel_name = 'parallel_sum__ssm_conv_s5__glu_gated_bilinear__prompt'

def setup_inputs():

    _w = (lambda a: (lambda t: t.to(torch.bfloat16) if t.is_floating_point() else t)(torch.as_tensor(a)))
    def _A_setup():
        B, S, D = 1, 2048, 512
        P = D
        Lambda_re = -0.5 * (lambda _x: torch.exp(torch.as_tensor(_x)))((lambda *s: torch.randn(*s, dtype=torch.bfloat16) * 0.2)(P))
        Lambda_im = (lambda *s: torch.randn(*s, dtype=torch.bfloat16) * 0.8)(P) * 1.0
        B_re = (lambda *s: torch.randn(*s, dtype=torch.bfloat16) * 0.2)(P, D) * (D ** -0.5)
        B_im = (lambda *s: torch.randn(*s, dtype=torch.bfloat16) * 0.2)(P, D) * (D ** -0.5)
        C_re = (lambda *s: torch.randn(*s, dtype=torch.bfloat16) * 0.2)(D, P) * (P ** -0.5)
        C_im = (lambda *s: torch.randn(*s, dtype=torch.bfloat16) * 0.2)(D, P) * (P ** -0.5)
        log_dt = (lambda *s: torch.randn(*s, dtype=torch.bfloat16) * 0.2)(P) * 0.3 - 2.0
        Dskip = (lambda *s: torch.randn(*s, dtype=torch.bfloat16) * 0.05)(D)
        return {
            "x": _w((lambda *s: torch.randn(*s, dtype=torch.bfloat16) * 0.05)(B, S, D)),
            "Lambda_re": _w(Lambda_re),
            "Lambda_im": _w(Lambda_im),
            "B_re": _w(B_re),
            "B_im": _w(B_im),
            "C_re": _w(C_re),
            "C_im": _w(C_im),
            "log_dt": _w(log_dt),
            "Dskip": _w(Dskip),
        }
    _A_in = _A_setup()

    def _B_setup():
        B, S, D, F = 1, 2048, 512, 1792
        return {
            "x": _w((lambda *s: torch.randn(*s, dtype=torch.bfloat16) * 0.05)(B, S, D)),
            "w_gate": _w((lambda *s: torch.randn(*s, dtype=torch.bfloat16) * 0.4)(D, F) * (D ** -0.5)),
            "w_up": _w((lambda *s: torch.randn(*s, dtype=torch.bfloat16) * 0.4)(D, F) * (D ** -0.5)),
            "w_down": _w((lambda *s: torch.randn(*s, dtype=torch.bfloat16) * 0.4)(F, D) * (F ** -0.5)),
        }
    _B_in = _B_setup()

    return {
        "A_x": _A_in["x"],
        "A_Lambda_re": _A_in["Lambda_re"],
        "A_Lambda_im": _A_in["Lambda_im"],
        "A_B_re": _A_in["B_re"],
        "A_B_im": _A_in["B_im"],
        "A_C_re": _A_in["C_re"],
        "A_C_im": _A_in["C_im"],
        "A_log_dt": _A_in["log_dt"],
        "A_Dskip": _A_in["Dskip"],
        "B_x": _B_in["x"],
        "B_w_gate": _B_in["w_gate"],
        "B_w_up": _B_in["w_up"],
        "B_w_down": _B_in["w_down"],
    }

def reference(A_x, A_Lambda_re, A_Lambda_im, A_B_re, A_B_im, A_C_re, A_C_im, A_log_dt, A_Dskip, B_x, B_w_gate, B_w_up, B_w_down):
    def _A_ref(x, Lambda_re, Lambda_im, B_re, B_im, C_re, C_im, log_dt, Dskip):
        Bsz, L, H = x.shape
        Lam = Lambda_re + 1j * Lambda_im
        Bmat = B_re + 1j * B_im
        Cmat = C_re + 1j * C_im
        dt = torch.exp(log_dt)
        Lam_bar = torch.exp(dt * Lam)
        B_bar = ((Lam_bar - 1.0) / Lam)[:, None] * Bmat
        Bu = torch.einsum("bld,pd->blp", x + 0j, B_bar)
        h = torch.zeros_like(Bu[:, 0, :])
        hs = []
        for t in range(L):
            h = Lam_bar[None, :] * h + Bu[:, t, :]
            hs.append(h)
        hseq = torch.stack(hs, 1)
        y = 2.0 * torch.einsum("blp,dp->bld", hseq, Cmat).real + x * Dskip[None, None, :]
        return y

    def _B_ref(x, w_gate, w_up, w_down):
        g = torch.einsum("bsd,df->bsf", x, w_gate)
        u = torch.einsum("bsd,df->bsf", x, w_up)
        h = g * u
        return torch.einsum("bsf,fd->bsd", h, w_down)

    x = A_x
    xn = _layernorm(x, axis=-1)
    return x + _A_ref(xn, A_Lambda_re, A_Lambda_im, A_B_re, A_B_im, A_C_re, A_C_im, A_log_dt, A_Dskip) + _B_ref(xn, B_w_gate, B_w_up, B_w_down)
```

```cpp
#include <hip/hip_runtime.h>
#include <hip/hip_bf16.h>
#include <stdint.h>

#define MIMIC 0

typedef unsigned short u16;
__device__ __forceinline__ float b2f(u16 v) { return __uint_as_float(((unsigned)v) << 16); }
__device__ __forceinline__ u16 f2b(float f) { unsigned u = __float_as_uint(f); u += 0x7fffu + ((u >> 16) & 1u); return (u16)(u >> 16); }
__device__ __forceinline__ float rb(float f) { return b2f(f2b(f)); }

constexpr int S = 2048, D = 512, P = 512, F = 1792;

__global__ void ln_kernel(const u16* x, float* xn) {
  int row = blockIdx.x; int t = threadIdx.x;
  float v[8]; float s = 0;
  for (int i = 0; i < 8; ++i) { v[i] = b2f(x[row * D + t * 8 + i]); s += v[i]; }
  for (int o = 32; o; o >>= 1) s += __shfl_xor(s, o);
  float m = s / D; float q = 0;
  for (int i = 0; i < 8; ++i) { float d = v[i] - m; q += d * d; }
  for (int o = 32; o; o >>= 1) q += __shfl_xor(q, o);
  float var = q / D;
  float sd = sqrtf(var + 1e-5f);
  for (int i = 0; i < 8; ++i) xn[row * D + t * 8 + i] = (v[i] - m) / sd;
}

__global__ void bu_kernel(const float* xn, const u16* Bre, const u16* Bim, const u16* Lre, const u16* Lim, const u16* logdt, float2* Bu) {
  int t = blockIdx.x; int p = threadIdx.x;
  __shared__ float xs[D];
  xs[p] = xn[t * D + p]; __syncthreads();
  float ar = 0, ai = 0;
  for (int d = 0; d < D; ++d) { ar += xs[d] * b2f(Bre[p * D + d]); ai += xs[d] * b2f(Bim[p * D + d]); }
  float dt = expf(b2f(logdt[p]));
  float lr = b2f(Lre[p]), li = b2f(Lim[p]);
  float e = expf(dt * lr); float br = e * cosf(dt * li), bi = e * sinf(dt * li);
  float nr = br - 1.f, ni = bi; float den = lr * lr + li * li;
  float cr = (nr * lr + ni * li) / den, ci = (ni * lr - nr * li) / den;
  Bu[t * P + p] = make_float2(cr * ar - ci * ai, cr * ai + ci * ar);
}

__global__ void scan_kernel(float2* Bu, const u16* Lre, const u16* Lim, const u16* logdt) {
  int p = blockIdx.x * 64 + threadIdx.x;
  float dt = expf(b2f(logdt[p]));
  float lr = b2f(Lre[p]), li = b2f(Lim[p]);
  float e = expf(dt * lr); float br = e * cosf(dt * li), bi = e * sinf(dt * li);
  float hr = 0, hi = 0;
  for (int t = 0; t < S; ++t) {
    float2 u = Bu[t * P + p];
    float nr = br * hr - bi * hi + u.x, ni = br * hi + bi * hr + u.y;
    hr = nr; hi = ni; Bu[t * P + p] = make_float2(hr, hi);
  }
}

__global__ void glu_kernel(const float* xn, const u16* wg, const u16* wu, float* h) {
  int t = blockIdx.x; __shared__ float xs[D];
  for (int i = threadIdx.x; i < D; i += 256) xs[i] = xn[t * D + i];
  __syncthreads();
  for (int f = threadIdx.x; f < F; f += 256) {
    float g = 0, u = 0;
    for (int d = 0; d < D; ++d) { g += xs[d] * b2f(wg[d * F + f]); u += xs[d] * b2f(wu[d * F + f]); }
    h[t * F + f] = g * u;
  }
}

__global__ void out_kernel(const u16* x, const float* xn, const float2* hs, const float* h, const u16* Cre, const u16* Cim, const u16* Dsk, const u16* wd, float* out) {
  int t = blockIdx.x; int d = threadIdx.x;
  __shared__ float2 hss[P]; __shared__ float hh[F];
  hss[d] = hs[t * P + d];
  for (int i = d; i < F; i += 512) hh[i] = h[t * F + i];
  __syncthreads();
  float y = 0;
  for (int p = 0; p < P; ++p) y += hss[p].x * b2f(Cre[d * P + p]) - hss[p].y * b2f(Cim[d * P + p]);
  float o = 0;
  for (int f = 0; f < F; ++f) o += hh[f] * b2f(wd[f * D + d]);
  float sk = xn[t * D + d] * b2f(Dsk[d]);
  out[t * D + d] = b2f(x[t * D + d]) + (2.f * y + sk) + o;
}

extern "C" void kernel_launch(void* const* d_in, const int* in_sizes, int n_in, void* d_out, int out_size, void* d_ws, size_t ws_size, hipStream_t stream) {
  const u16* x = (const u16*)d_in[0]; const u16* Lre = (const u16*)d_in[1]; const u16* Lim = (const u16*)d_in[2];
  const u16* Bre = (const u16*)d_in[3]; const u16* Bim = (const u16*)d_in[4]; const u16* Cre = (const u16*)d_in[5]; const u16* Cim = (const u16*)d_in[6];
  const u16* logdt = (const u16*)d_in[7]; const u16* Dsk = (const u16*)d_in[8];
  const u16* wg = (const u16*)d_in[10]; const u16* wu = (const u16*)d_in[11]; const u16* wd = (const u16*)d_in[12];
  float* xn = (float*)d_ws; float2* Bu = (float2*)(xn + S * D); float* h = (float*)(Bu + S * P);
  ln_kernel<<<S, 64, 0, stream>>>(x, xn);
  bu_kernel<<<S, 512, 0, stream>>>(xn, Bre, Bim, Lre, Lim, logdt, Bu);
  scan_kernel<<<P / 64, 64, 0, stream>>>(Bu, Lre, Lim, logdt);
  glu_kernel<<<S, 256, 0, stream>>>(xn, wg, wu, h);
  out_kernel<<<S, 512, 0, stream>>>(x, xn, Bu, h, Cre, Cim, Dsk, wd, (float*)d_out);
}
```

```cpp
#include <hip/hip_runtime.h>
#include <stdint.h>

typedef unsigned u32;
#define BUF(n) const void* n, u32 n##_bytes, u32 n##_fmt

__global__ __launch_bounds__(256, 1) __attribute__((aligned(4096))) void s5_glu_fused(BUF(x), BUF(bre), BUF(bim), BUF(wg), BUF(wu), BUF(wsel), BUF(wd), BUF(cre), BUF(cim),
                                                        BUF(ws), BUF(out), const void* lre, const void* lim, const void* ldt, const void* dsk) {
  __shared__ __attribute__((aligned(16))) char smem[163840];
  u32 tid = threadIdx.x, bid = blockIdx.x;
  uint64_t karg = (uint64_t)(uintptr_t)__builtin_amdgcn_kernarg_segment_ptr();
  asm volatile(
    "s_load_dwordx16 s[8:23], s[0:1], 0\n\t"
    "s_load_dwordx8 s[72:79], s[0:1], 176\n\t"
    "s_load_dwordx16 s[24:39], s[0:1], 64\n\t"
    "s_load_dwordx8 s[40:47], s[0:1], 128\n\t"
    "s_load_dwordx4 s[48:51], s[0:1], 160\n\t"
    "s_getpc_b64 s[4:5]\n\t"
    "Lpc_%=:\n\t"
    "v_lshrrev_b32 v73, 2, v0\n\t"
    "v_lshlrev_b32 v73, 7, v73\n\t"
    "global_load_dword a53, v73, s[4:5]\n\t"
    "s_lshr_b32 s6, s2, 3\n\t"
    "s_lshl_b32 s6, s6, 10\n\t"
    "v_lshrrev_b32 v73, 5, v0\n\t"
    "v_lshl_add_u32 v73, v73, 7, s6\n\t"
    "v_min_u32 v73, Lend_%=-Lpc_%=-4, v73\n\t"
    "global_load_dword a54, v73, s[4:5]\n\t"
    "v_lshrrev_b32 v74, 6, v0\n\t"
    "v_and_b32 v64, 63, v0\n\t"
    "v_readfirstlane_b32 s95, v74\n\t"
    "s_bfe_u32 s92, s2, 0x30003\n\t"
    "s_and_b32 s4, s2, 1\n\t"
    "s_lshl3_add_u32 s92, s4, s92\n\t"
    "s_bfe_u32 s93, s2, 0x20001\n\t"
    "s_lshr_b32 s94, s2, 6\n\t"
    "s_add_u32 s4, s93, s92\n\t"
    "s_lshl_b32 s4, s4, 8\n\t"
    "s_and_b32 s56, s4, 0x380\n\t"
    "s_addk_i32 s4, 128\n\t"
    "s_and_b32 s57, s4, 0x380\n\t"
    "s_addk_i32 s4, 128\n\t"
    "s_and_b32 s58, s4, 0x380\n\t"
    "v_lshrrev_b32 v65, 5, v64\n\t"
    "v_bfe_u32 v74, v64, 3, 2\n\t"
    "v_lshl_or_b32 v74, v65, 4, v74\n\t"
    "s_lshl2_add_u32 s5, s92, s95\n\t"
    "s_lshl_b32 s5, s5, 15\n\t"
    "v_lshrrev_b32 v75, 4, v64\n\t"
    "v_xor_b32 v75, v75, v64\n\t"
    "v_and_b32 v75, 7, v75\n\t"
    "v_lshlrev_b32 v68, 4, v75\n\t"
    "v_lshl_add_u32 v74, v74, 10, s5\n\t"
    "v_or_b32 v32, v74, v68\n\t"
    "v_add_u32 v33, 3072, v32\n\t"
    "v_xor_b32 v74, 64, v32\n\t"
    "v_add_u32 v34, 6144, v74\n\t"
    "v_add_u32 v35, 9216, v74\n\t"
    "s_mul_i32 s52, s95, 38976\n\t"
    "s_addk_i32 s52, 7936\n\t"
    "v_and_b32 v66, 31, v64\n\t"
    "s_lshl2_add_u32 s6, s93, s94\n\t"
    "s_lshl_b32 s7, s6, 6\n\t"
    "v_lshl_add_u32 v74, v66, 1, s7\n\t"
    "s_lshl2_add_u32 s7, s94, s93\n\t"
    "s_lshl_b32 s7, s7, 6\n\t"
    "v_lshl_add_u32 v75, v66, 1, s7\n\t"
    "v_mov_b32 v76, 655360\n\t"
    "s_waitcnt lgkmcnt(0)\n\t"
    "s_mov_b32 m0, s52\n\t"
    "s_nop 0\n\t"
    "buffer_load_dwordx4 v32, s[8:11], s56 offen lds\n\t"
    "buffer_load_dwordx4 v33, s[8:11], s56 offen offset:1024 lds\n\t"
    "buffer_load_dwordx4 v34, s[8:11], s56 offen offset:2048 lds\n\t"
    "buffer_load_dwordx4 v35, s[8:11], s56 offen offset:3072 lds\n\t"
    "global_load_ushort v80, v74, s[72:73]\n\t"
    "global_load_ushort v81, v74, s[74:75]\n\t"
    "global_load_ushort v82, v74, s[76:77]\n\t"
    "global_load_ushort v83, v75, s[78:79]\n\t"
    "buffer_load_dword v84, v76, s[44:47], 0 offen sc1\n\t"
    "s_add_u32 m0, s52, 4096\n\t"
    "s_nop 0\n\t"
    "buffer_load_dwordx4 v32, s[8:11], s57 offen lds\n\t"
    "buffer_load_dwordx4 v33, s[8:11], s57 offen offset:1024 lds\n\t"
    "buffer_load_dwordx4 v34, s[8:11], s57 offen offset:2048 lds\n\t"
    "buffer_load_dwordx4 v35, s[8:11], s57 offen offset:3072 lds\n\t"
    "s_add_u32 m0, s52, 8192\n\t"
    "s_nop 0\n\t"
    "buffer_load_dwordx4 v32, s[8:11], s58 offen lds\n\t"
    "buffer_load_dwordx4 v33, s[8:11], s58 offen offset:1024 lds\n\t"
    "buffer_load_dwordx4 v34, s[8:11], s58 offen offset:2048 lds\n\t"
    "buffer_load_dwordx4 v35, s[8:11], s58 offen offset:3072 lds\n\t"
    "s_addk_i32 s4, 128\n\t"
    "s_and_b32 s59, s4, 0x380\n\t"
    "s_addk_i32 s4, 128\n\t"
    "s_and_b32 s60, s4, 0x380\n\t"
    "s_addk_i32 s4, 128\n\t"
    "s_and_b32 s61, s4, 0x380\n\t"
    "s_addk_i32 s4, 128\n\t"
    "s_and_b32 s62, s4, 0x380\n\t"
    "s_addk_i32 s4, 128\n\t"
    "s_and_b32 s63, s4, 0x380\n\t"
    "s_lshl4_add_u32 s96, s92, s6\n\t"
    "s_mul_i32 s64, s56, 1792\n\t"
    "s_mul_i32 s65, s57, 1792\n\t"
    "s_mul_i32 s66, s58, 1792\n\t"
    "s_mul_i32 s67, s59, 1792\n\t"
    "s_mul_i32 s68, s60, 1792\n\t"
    "s_mul_i32 s69, s61, 1792\n\t"
    "s_mul_i32 s70, s62, 1792\n\t"
    "s_mul_i32 s71, s63, 1792\n\t"
    "s_sub_u32 s36, s36, 3072\n\t"
    "s_subb_u32 s37, s37, 0\n\t"
    "s_sub_u32 s40, s40, 3072\n\t"
    "s_subb_u32 s41, s41, 0\n\t"
    "s_sub_u32 s16, s16, 1024\n\t"
    "s_subb_u32 s17, s17, 0\n\t"
    "s_sub_u32 s24, s24, 2048\n\t"
    "s_subb_u32 s25, s25, 0\n\t"
    "v_lshlrev_b32 v0, 4, v64\n\t"
    "v_lshrrev_b32 v67, 3, v64\n\t"
    "v_bfe_u32 v69, v64, 2, 2\n\t"
    "v_and_b32 v70, 3, v64\n\t"
    "v_lshlrev_b32 v70, 3, v70\n\t"
    "v_bfe_u32 v71, v64, 4, 1\n\t"
    "s_lshl_b32 s4, s95, 1\n\t"
    "v_add_u32 v72, s4, v65\n\t"
    "v_bfe_u32 v73, v66, 1, 2\n\t"
    "v_xor_b32 v73, v73, v65\n\t"
    "v_lshl_or_b32 v74, v71, 2, v73\n\t"
    "v_lshlrev_b32 v74, 4, v74\n\t"
    "v_lshl_add_u32 v75, v66, 7, s52\n\t"
    "v_add_u32 v36, v74, v75\n\t"
    "v_xad_u32 v37, v74, 32, v75\n\t"
    "v_xad_u32 v38, v74, 64, v75\n\t"
    "v_xor_b32 v74, 96, v74\n\t"
    "v_add_u32 v39, v74, v75\n\t"
    "v_lshrrev_b32 v74, 3, v66\n\t"
    "v_mul_u32_u24 v74, 38976, v74\n\t"
    "v_and_b32 v75, 7, v66\n\t"
    "v_lshl_add_u32 v74, v75, 7, v74\n\t"
    "v_lshl_add_u32 v2, v73, 4, v74\n\t"
    "v_xor_b32 v3, 32, v2\n\t"
    "v_lshl_or_b32 v76, v71, 5, v70\n\t"
    "v_mul_u32_u24 v74, 38976, v69\n\t"
    "v_lshl_add_u32 v74, v65, 8, v74\n\t"
    "v_add_u32 v1, v74, v76\n\t"
    "v_lshl_or_b32 v73, v67, 10, v68\n\t"
    "v_lshrrev_b32 v77, 3, v64\n\t"
    "s_lshl2_add_u32 s97, s6, s95\n\t"
    "s_lshl_b32 s97, s97, 13\n\t"
    "v_lshl_or_b32 v78, v77, 10, v68\n\t"
    "v_add_u32 v4, s97, v78\n\t"
    "s_mul_i32 s80, s93, 896\n\t"
    "s_mul_i32 s97, s95, 3584\n\t"
    "s_add_u32 s97, s97, s80\n\t"
    "s_lshl_b32 s80, s94, 7\n\t"
    "s_add_u32 s97, s97, s80\n\t"
    "v_mul_u32_u24 v77, 14336, v77\n\t"
    "v_and_b32 v78, 7, v64\n\t"
    "v_lshlrev_b32 v78, 4, v78\n\t"
    "v_add3_u32 v5, v77, v78, s97\n\t"
    "v_add_u32 v6, 113664, v5\n\t"
    "s_cmp_eq_u32 s94, 3\n\t"
    "s_cselect_b32 s97, 0x40000000, 0\n\t"
    "s_add_u32 s4, s97, 512\n\t"
    "v_add_u32 v8, s4, v5\n\t"
    "v_add_u32 v9, s4, v6\n\t"
    "v_or_b32 v30, s97, v0\n\t"
    "v_add_u32 v13, s52, v0\n\t"
    "v_lshl_or_b32 v18, v67, 26, v0\n\t"
    "s_lshl4_add_u32 s4, s94, s95\n\t"
    "s_lshl_b32 s4, s4, 13\n\t"
    "s_lshl_b32 s73, s93, 8\n\t"
    "s_add_u32 s4, s4, s73\n\t"
    "s_lshl_b32 s73, s94, 6\n\t"
    "v_xor_b32 v73, s73, v73\n\t"
    "v_add_u32 v11, s4, v73\n\t"
    "v_xor_b32 v12, 0x80, v11\n\t"
    "s_lshl2_add_u32 s4, s95, s94\n\t"
    "s_lshl_b32 s4, s4, 8\n\t"
    "s_lshl_b32 s73, s93, 6\n\t"
    "v_add_u32 v73, s73, v0\n\t"
    "v_and_b32 v73, 0xf0, v73\n\t"
    "v_lshrrev_b32 v74, 4, v64\n\t"
    "v_lshl_or_b32 v73, v74, 12, v73\n\t"
    "v_add_u32 v10, s4, v73\n\t"
    "v_lshlrev_b32 v73, 3, v66\n\t"
    "v_lshl_add_u32 v73, v72, 8, v73\n\t"
    "v_mov_b32 v40, 0\n\t"
    "v_mov_b32 v41, 0\n\t"
    "ds_write_b64 v73, v[40:41]\n\t"
    "v_accvgpr_write_b32 a44, v73\n\t"
    "s_lshl4_add_u32 s4, s92, s96\n\t"
    "s_lshl_b32 s4, s4, 9\n\t"
    "v_lshl_add_u32 v73, v66, 4, s4\n\t"
    "v_sub_u32 v75, 7, v72\n\t"
    "v_lshl_add_u32 v75, v75, 26, v73\n\t"
    "v_accvgpr_write_b32 a45, v75\n\t"
    "v_add_u32 v75, 1, v72\n\t"
    "v_lshlrev_b32 v75, 14, v75\n\t"
    "v_sub_u32 v73, v73, v75\n\t"
    "v_accvgpr_write_b32 a46, v73\n\t"
    "s_add_u32 s4, s52, 29696\n\t"
    "v_lshlrev_b32 v73, 3, v65\n\t"
    "v_mul_u32_u24 v75, 72, v66\n\t"
    "v_add3_u32 v75, v75, v73, s4\n\t"
    "v_accvgpr_write_b32 a47, v75\n\t"
    "v_add_u32 v73, v73, v69\n\t"
    "v_mul_u32_u24 v73, 72, v73\n\t"
    "v_add3_u32 v73, v73, v76, s4\n\t"
    "v_accvgpr_write_b32 a48, v73\n\t"
    "s_lshl2_add_u32 s96, s96, s95\n\t"
    "v_add_u32 v74, 1, v74\n\t"
    "v_min_u32 v74, 3, v74\n\t"
    "v_lshlrev_b32 v75, 2, v74\n\t"
    "v_xor_b32 v75, s96, v75\n\t"
    "v_lshlrev_b32 v19, 7, v75\n\t"
    "v_lshlrev_b32 v75, 4, v74\n\t"
    "v_xor_b32 v75, s96, v75\n\t"
    "v_lshlrev_b32 v75, 7, v75\n\t"
    "v_accvgpr_write_b32 a49, v75\n\t"
    "s_add_u32 s4, s5, s7\n\t"
    "v_lshlrev_b32 v73, 14, v65\n\t"
    "v_lshl_add_u32 v73, v66, 1, v73\n\t"
    "v_add_u32 v73, s4, v73\n\t"
    "v_lshlrev_b32 v73, 1, v73\n\t"
    "v_accvgpr_write_b32 a50, v73\n\t"
    "s_and_b32 s31, s7, 0x380\n\t"
    "s_bitcmp1_b32 s93, 0\n\t"
    "s_cselect_b64 vcc, -1, 0\n\t"
    "v_cndmask_b32 v42, v36, v38, vcc\n\t"
    "v_cndmask_b32 v43, v37, v39, vcc\n\t"
    "s_mul_i32 s4, s93, 458752\n\t"
    "s_xor_b32 s5, s94, 0\n\t"
    "s_lshl_b32 s72, s5, 16\n\t"
    "s_add_u32 s72, s72, s4\n\t"
    "s_xor_b32 s5, s94, 1\n\t"
    "s_lshl_b32 s73, s5, 16\n\t"
    "s_add_u32 s73, s73, s4\n\t"
    "s_xor_b32 s5, s94, 2\n\t"
    "s_lshl_b32 s74, s5, 16\n\t"
    "s_add_u32 s74, s74, s4\n\t"
    "s_xor_b32 s5, s94, 3\n\t"
    "s_lshl_b32 s75, s5, 16\n\t"
    "s_add_u32 s75, s75, s4\n\t"
    "s_add_u32 s4, s4, 262144\n\t"
    "s_andn2_b32 s7, s96, 12\n\t"
    "s_add_u32 s5, s94, 0\n\t"
    "s_cmp_ge_u32 s5, 3\n\t"
    "s_cselect_b32 s97, 3, 0\n\t"
    "s_sub_u32 s5, s5, s97\n\t"
    "s_lshl_b32 s76, s5, 16\n\t"
    "s_add_u32 s76, s76, s4\n\t"
    "s_lshl2_add_u32 s5, s5, s7\n\t"
    "s_mul_i32 s5, s5, 12288\n\t"
    "s_add_u32 s28, s5, 8392704\n\t"
    "s_add_u32 s5, s94, 1\n\t"
    "s_cmp_ge_u32 s5, 3\n\t"
    "s_cselect_b32 s97, 3, 0\n\t"
    "s_sub_u32 s5, s5, s97\n\t"
    "s_lshl_b32 s77, s5, 16\n\t"
    "s_add_u32 s77, s77, s4\n\t"
    "s_lshl2_add_u32 s5, s5, s7\n\t"
    "s_mul_i32 s5, s5, 12288\n\t"
    "s_add_u32 s29, s5, 8392704\n\t"
    "s_add_u32 s5, s94, 2\n\t"
    "s_cmp_ge_u32 s5, 3\n\t"
    "s_cselect_b32 s97, 3, 0\n\t"
    "s_sub_u32 s5, s5, s97\n\t"
    "s_lshl_b32 s78, s5, 16\n\t"
    "s_add_u32 s78, s78, s4\n\t"
    "s_lshl2_add_u32 s5, s5, s7\n\t"
    "s_mul_i32 s5, s5, 12288\n\t"
    "s_add_u32 s30, s5, 8392704\n\t"
    "s_lshl2_add_u32 s6, s92, s94\n\t"
    "s_lshl2_add_u32 s6, s6, s95\n\t"
    "s_mul_i32 s6, s6, 24576\n\t"
    "s_add_u32 s4, s93, 0\n\t"
    "s_and_b32 s4, s4, 3\n\t"
    "s_lshl_b32 s80, s4, 15\n\t"
    "s_addk_i32 s80, 3072\n\t"
    "s_mul_i32 s4, s4, 6144\n\t"
    "s_add_u32 s4, s4, s6\n\t"
    "s_add_u32 s91, s4, 2097152\n\t"
    "s_add_u32 s4, s93, 1\n\t"
    "s_and_b32 s4, s4, 3\n\t"
    "s_lshl_b32 s81, s4, 15\n\t"
    "s_addk_i32 s81, 2048\n\t"
    "s_mul_i32 s4, s4, 6144\n\t"
    "s_add_u32 s4, s4, s6\n\t"
    "s_add_u32 s88, s4, 2097152\n\t"
    "s_add_u32 s4, s93, 2\n\t"
    "s_and_b32 s4, s4, 3\n\t"
    "s_lshl_b32 s82, s4, 15\n\t"
    "s_addk_i32 s82, 1024\n\t"
    "s_mul_i32 s4, s4, 6144\n\t"
    "s_add_u32 s4, s4, s6\n\t"
    "s_add_u32 s89, s4, 2099200\n\t"
    "s_add_u32 s4, s93, 3\n\t"
    "s_and_b32 s4, s4, 3\n\t"
    "s_lshl_b32 s83, s4, 15\n\t"
    "s_mul_i32 s4, s4, 6144\n\t"
    "s_add_u32 s4, s4, s6\n\t"
    "s_add_u32 s90, s4, 2101248\n\t"
    "s_xor_b32 s4, s96, 0\n\t"
    "s_mul_i32 s4, s4, 12288\n\t"
    "s_add_u32 s55, s4, 8388608\n\t"
    "s_xor_b32 s4, s96, 4\n\t"
    "s_mul_i32 s4, s4, 12288\n\t"
    "s_add_u32 s84, s4, 8388608\n\t"
    "s_xor_b32 s4, s96, 8\n\t"
    "s_mul_i32 s4, s4, 12288\n\t"
    "s_add_u32 s85, s4, 8388608\n\t"
    "s_xor_b32 s4, s96, 12\n\t"
    "s_mul_i32 s4, s4, 12288\n\t"
    "s_add_u32 s86, s4, 8388608\n\t"
    "s_lshl_b32 s87, s96, 7\n\t"
    "s_add_u32 s87, s87, 1048576\n\t"
    "s_mov_b32 s6, 0xffff0000\n\t"
    "s_mov_b32 s7, 0x3f803f80\n\t"
    "s_movk_i32 s53, 0x3f80\n\t"
    "s_mov_b32 s54, 1.0\n\t"
    "v_mov_b32 v20, 0\n\t"
    "v_mov_b32 v21, 0\n\t"
    "v_mov_b32 v22, 0\n\t"
    "v_mov_b32 v23, 0\n\t"
    "s_add_u32 m0, s52, 12288\n\t"
    "s_nop 0\n\t"
    "buffer_load_dwordx4 v32, s[8:11], s59 offen lds\n\t"
    "buffer_load_dwordx4 v33, s[8:11], s59 offen offset:1024 lds\n\t"
    "buffer_load_dwordx4 v34, s[8:11], s59 offen offset:2048 lds\n\t"
    "buffer_load_dwordx4 v35, s[8:11], s59 offen offset:3072 lds\n\t"
    "s_waitcnt vmcnt(17)\n\t"
    "s_cmp_lg_u32 s31, s56\n\t"
    "s_cbranch_scc1 Lxs0_%=\n\t"
    "ds_read_b128 a[220:223], v42 offset:0\n\t"
    "ds_read_b128 a[224:227], v43 offset:0\n\t"
    "Lxs0_%=:\n\t"
    "ds_read_b128 v[128:131], v36 offset:0\n\t"
    "ds_read_b128 v[132:135], v37 offset:0\n\t"
    "ds_read_b128 v[136:139], v38 offset:0\n\t"
    "ds_read_b128 v[140:143], v39 offset:0\n\t"
    "s_add_u32 m0, s52, 16384\n\t"
    "s_nop 0\n\t"
    "buffer_load_dwordx4 v32, s[8:11], s60 offen lds\n\t"
    "s_waitcnt vmcnt(13)\n\t"
    "v_readfirstlane_b32 s2, v84\n\t"
    "v_accvgpr_write_b32 a52, v83\n\t"
    "v_lshlrev_b32 v82, 16, v82\n\t"
    "v_lshlrev_b32 v80, 16, v80\n\t"
    "v_lshlrev_b32 v81, 16, v81\n\t"
    "v_mul_f32 v82, 0x3fb8aa3b, v82\n\t"
    "buffer_load_dwordx4 v33, s[8:11], s60 offen offset:1024 lds\n\t"
    "v_exp_f32 v82, v82\n\t"
    "s_add_u32 s2, s2, 1\n\t"
    "s_xor_b32 s3, s2, 0x9e3779b9\n\t"
    "v_cvt_pk_bf16_f32 v82, v82, v82\n\t"
    "v_lshlrev_b32 v82, 16, v82\n\t"
    "v_accvgpr_write_b32 a56, s2\n\t"
    "buffer_load_dwordx4 v34, s[8:11], s60 offen offset:2048 lds\n\t"
    "v_accvgpr_write_b32 a57, s3\n\t"
    "v_accvgpr_write_b32 a58, s2\n\t"
    "v_accvgpr_write_b32 a59, s3\n\t"
    "v_mul_f32 v86, v82, v80\n\t"
    "v_mul_f32 v87, v82, v81\n\t"
    "v_mul_f32 v86, 0x3fb8aa3b, v86\n\t"
    "buffer_load_dwordx4 v35, s[8:11], s60 offen offset:3072 lds\n\t"
    "v_mul_f32 v87, 0.15915494, v87\n\t"
    "v_exp_f32 v86, v86\n\t"
    "v_rndne_f32 v88, v87\n\t"
    "v_sub_f32 v87, v87, v88\n\t"
    "v_cos_f32 v88, v87\n\t"
    "v_sin_f32 v87, v87\n\t"
    "v_mul_f32 v82, v80, v80\n\t"
    "s_waitcnt vmcnt(12)\n\t"
    "s_cmp_lg_u32 s31, s57\n\t"
    "s_cbranch_scc1 Lxs1_%=\n\t"
    "ds_read_b128 a[220:223], v42 offset:4096\n\t"
    "ds_read_b128 a[224:227], v43 offset:4096\n\t"
    "Lxs1_%=:\n\t"
    "ds_read_b128 v[144:147], v36 offset:4096\n\t"
    "ds_read_b128 v[148:151], v37 offset:4096\n\t"
    "ds_read_b128 v[152:155], v38 offset:4096\n\t"
    "ds_read_b128 v[156:159], v39 offset:4096\n\t"
    "s_add_u32 m0, s52, 20480\n\t"
    "s_nop 0\n\t"
    "buffer_load_dwordx4 v32, s[8:11], s61 offen lds\n\t"
    "s_waitcnt lgkmcnt(7)\n\t"
    "v_dot2c_f32_bf16 v20, s7, v128\n\t"
    "v_dot2c_f32_bf16 v22, v128, v128\n\t"
    "v_dot2c_f32_bf16 v21, s7, v129\n\t"
    "v_dot2c_f32_bf16 v23, v129, v129\n\t"
    "v_dot2c_f32_bf16 v20, s7, v130\n\t"
    "v_dot2c_f32_bf16 v22, v130, v130\n\t"
    "v_dot2c_f32_bf16 v21, s7, v131\n\t"
    "v_dot2c_f32_bf16 v23, v131, v131\n\t"
    "s_waitcnt lgkmcnt(6)\n\t"
    "v_dot2c_f32_bf16 v20, s7, v132\n\t"
    "v_dot2c_f32_bf16 v22, v132, v132\n\t"
    "v_dot2c_f32_bf16 v21, s7, v133\n\t"
    "v_dot2c_f32_bf16 v23, v133, v133\n\t"
    "v_dot2c_f32_bf16 v20, s7, v134\n\t"
    "v_dot2c_f32_bf16 v22, v134, v134\n\t"
    "buffer_load_dwordx4 v33, s[8:11], s61 offen offset:1024 lds\n\t"
    "v_dot2c_f32_bf16 v21, s7, v135\n\t"
    "v_dot2c_f32_bf16 v23, v135, v135\n\t"
    "s_waitcnt lgkmcnt(5)\n\t"
    "v_dot2c_f32_bf16 v20, s7, v136\n\t"
    "v_dot2c_f32_bf16 v22, v136, v136\n\t"
    "v_dot2c_f32_bf16 v21, s7, v137\n\t"
    "v_dot2c_f32_bf16 v23, v137, v137\n\t"
    "v_dot2c_f32_bf16 v20, s7, v138\n\t"
    "v_dot2c_f32_bf16 v22, v138, v138\n\t"
    "v_dot2c_f32_bf16 v21, s7, v139\n\t"
    "v_dot2c_f32_bf16 v23, v139, v139\n\t"
    "s_waitcnt lgkmcnt(4)\n\t"
    "v_dot2c_f32_bf16 v20, s7, v140\n\t"
    "v_dot2c_f32_bf16 v22, v140, v140\n\t"
    "v_dot2c_f32_bf16 v21, s7, v141\n\t"
    "v_dot2c_f32_bf16 v23, v141, v141\n\t"
    "buffer_load_dwordx4 v34, s[8:11], s61 offen offset:2048 lds\n\t"
    "v_dot2c_f32_bf16 v20, s7, v142\n\t"
    "v_dot2c_f32_bf16 v22, v142, v142\n\t"
    "v_dot2c_f32_bf16 v21, s7, v143\n\t"
    "v_dot2c_f32_bf16 v23, v143, v143\n\t"
    "v_mul_f32 v90, v86, v88\n\t"
    "v_mul_f32 v91, v86, v87\n\t"
    "v_fmac_f32 v82, v81, v81\n\t"
    "v_rcp_f32 v82, v82\n\t"
    "v_add_f32 v86, -1.0, v90\n\t"
    "v_mul_f32 v87, v86, v80\n\t"
    "v_mul_f32 v88, v91, v80\n\t"
    "v_fmac_f32 v87, v91, v81\n\t"
    "v_fma_f32 v88, -v86, v81, v88\n\t"
    "v_add_f32 v82, v82, v82\n\t"
    "buffer_load_dwordx4 v35, s[8:11], s61 offen offset:3072 lds\n\t"
    "v_mul_f32 v87, v87, v82\n\t"
    "v_mul_f32 v88, v88, v82\n\t"
    "v_accvgpr_write_b32 a34, v87\n\t"
    "v_accvgpr_write_b32 a35, v88\n\t"
    "v_accvgpr_write_b32 a32, v90\n\t"
    "v_accvgpr_write_b32 a33, v91\n\t"
    "v_mul_f32 v92, v90, v90\n\t"
    "v_mul_f32 v93, v90, v91\n\t"
    "v_fma_f32 v92, -v91, v91, v92\n\t"
    "v_add_f32 v93, v93, v93\n\t"
    "v_mul_f32 v94, v92, v92\n\t"
    "v_mul_f32 v95, v92, v93\n\t"
    "v_fma_f32 v94, -v93, v93, v94\n\t"
    "v_add_f32 v95, v95, v95\n\t"
    "v_mul_f32 v96, v94, v94\n\t"
    "s_waitcnt vmcnt(12)\n\t"
    "s_cmp_lg_u32 s31, s58\n\t"
    "s_cbranch_scc1 Lxs2_%=\n\t"
    "ds_read_b128 a[220:223], v42 offset:8192\n\t"
    "ds_read_b128 a[224:227], v43 offset:8192\n\t"
    "Lxs2_%=:\n\t"
    "ds_read_b128 v[160:163], v36 offset:8192\n\t"
    "ds_read_b128 v[164:167], v37 offset:8192\n\t"
    "ds_read_b128 v[168:171], v38 offset:8192\n\t"
    "ds_read_b128 v[172:175], v39 offset:8192\n\t"
    "s_add_u32 m0, s52, 24576\n\t"
    "s_nop 0\n\t"
    "buffer_load_dwordx4 v32, s[8:11], s62 offen lds\n\t"
    "s_waitcnt lgkmcnt(7)\n\t"
    "v_dot2c_f32_bf16 v20, s7, v144\n\t"
    "v_dot2c_f32_bf16 v22, v144, v144\n\t"
    "v_dot2c_f32_bf16 v21, s7, v145\n\t"
    "v_dot2c_f32_bf16 v23, v145, v145\n\t"
    "v_dot2c_f32_bf16 v20, s7, v146\n\t"
    "v_dot2c_f32_bf16 v22, v146, v146\n\t"
    "v_dot2c_f32_bf16 v21, s7, v147\n\t"
    "v_dot2c_f32_bf16 v23, v147, v147\n\t"
    "s_waitcnt lgkmcnt(6)\n\t"
    "v_dot2c_f32_bf16 v20, s7, v148\n\t"
    "v_dot2c_f32_bf16 v22, v148, v148\n\t"
    "v_dot2c_f32_bf16 v21, s7, v149\n\t"
    "v_dot2c_f32_bf16 v23, v149, v149\n\t"
    "v_dot2c_f32_bf16 v20, s7, v150\n\t"
    "v_dot2c_f32_bf16 v22, v150, v150\n\t"
    "buffer_load_dwordx4 v33, s[8:11], s62 offen offset:1024 lds\n\t"
    "v_dot2c_f32_bf16 v21, s7, v151\n\t"
    "v_dot2c_f32_bf16 v23, v151, v151\n\t"
    "s_waitcnt lgkmcnt(5)\n\t"
    "v_dot2c_f32_bf16 v20, s7, v152\n\t"
    "v_dot2c_f32_bf16 v22, v152, v152\n\t"
    "v_dot2c_f32_bf16 v21, s7, v153\n\t"
    "v_dot2c_f32_bf16 v23, v153, v153\n\t"
    "v_dot2c_f32_bf16 v20, s7, v154\n\t"
    "v_dot2c_f32_bf16 v22, v154, v154\n\t"
    "v_dot2c_f32_bf16 v21, s7, v155\n\t"
    "v_dot2c_f32_bf16 v23, v155, v155\n\t"
    "s_waitcnt lgkmcnt(4)\n\t"
    "v_dot2c_f32_bf16 v20, s7, v156\n\t"
    "v_dot2c_f32_bf16 v22, v156, v156\n\t"
    "v_dot2c_f32_bf16 v21, s7, v157\n\t"
    "v_dot2c_f32_bf16 v23, v157, v157\n\t"
    "buffer_load_dwordx4 v34, s[8:11], s62 offen offset:2048 lds\n\t"
    "v_dot2c_f32_bf16 v20, s7, v158\n\t"
    "v_dot2c_f32_bf16 v22, v158, v158\n\t"
    "v_dot2c_f32_bf16 v21, s7, v159\n\t"
    "v_dot2c_f32_bf16 v23, v159, v159\n\t"
    "v_mul_f32 v97, v94, v95\n\t"
    "v_fma_f32 v96, -v95, v95, v96\n\t"
    "v_add_f32 v97, v97, v97\n\t"
    "v_mul_f32 v98, v96, v96\n\t"
    "v_mul_f32 v99, v96, v97\n\t"
    "v_fma_f32 v98, -v97, v97, v98\n\t"
    "v_add_f32 v99, v99, v99\n\t"
    "v_mul_f32 v100, v98, v98\n\t"
    "v_mul_f32 v101, v98, v99\n\t"
    "v_fma_f32 v100, -v99, v99, v100\n\t"
    "buffer_load_dwordx4 v35, s[8:11], s62 offen offset:3072 lds\n\t"
    "v_add_f32 v101, v101, v101\n\t"
    "v_mul_f32 v102, v100, v100\n\t"
    "v_mul_f32 v103, v100, v101\n\t"
    "v_fma_f32 v102, -v101, v101, v102\n\t"
    "v_add_f32 v103, v103, v103\n\t"
    "v_mul_f32 v104, v102, v102\n\t"
    "v_mul_f32 v105, v102, v103\n\t"
    "v_fma_f32 v104, -v103, v103, v104\n\t"
    "v_add_f32 v105, v105, v105\n\t"
    "v_mul_f32 v106, v104, v104\n\t"
    "v_mul_f32 v107, v104, v105\n\t"
    "v_fma_f32 v106, -v105, v105, v106\n\t"
    "v_add_f32 v107, v107, v107\n\t"
    "v_mul_f32 v108, v106, v106\n\t"
    "v_mul_f32 v109, v106, v107\n\t"
    "s_waitcnt vmcnt(12)\n\t"
    "s_cmp_lg_u32 s31, s59\n\t"
    "s_cbranch_scc1 Lxs3_%=\n\t"
    "ds_read_b128 a[220:223], v42 offset:12288\n\t"
    "ds_read_b128 a[224:227], v43 offset:12288\n\t"
    "Lxs3_%=:\n\t"
    "ds_read_b128 v[176:179], v36 offset:12288\n\t"
    "ds_read_b128 v[180:183], v37 offset:12288\n\t"
    "ds_read_b128 v[184:187], v38 offset:12288\n\t"
    "ds_read_b128 v[188:191], v39 offset:12288\n\t"
    "s_add_u32 m0, s52, 28672\n\t"
    "s_nop 0\n\t"
    "buffer_load_dwordx4 v32, s[8:11], s63 offen lds\n\t"
    "s_waitcnt lgkmcnt(7)\n\t"
    "v_dot2c_f32_bf16 v20, s7, v160\n\t"
    "v_dot2c_f32_bf16 v22, v160, v160\n\t"
    "v_dot2c_f32_bf16 v21, s7, v161\n\t"
    "v_dot2c_f32_bf16 v23, v161, v161\n\t"
    "v_dot2c_f32_bf16 v20, s7, v162\n\t"
    "v_dot2c_f32_bf16 v22, v162, v162\n\t"
    "v_dot2c_f32_bf16 v21, s7, v163\n\t"
    "v_dot2c_f32_bf16 v23, v163, v163\n\t"
    "s_waitcnt lgkmcnt(6)\n\t"
    "v_dot2c_f32_bf16 v20, s7, v164\n\t"
    "v_dot2c_f32_bf16 v22, v164, v164\n\t"
    "v_dot2c_f32_bf16 v21, s7, v165\n\t"
    "v_dot2c_f32_bf16 v23, v165, v165\n\t"
    "v_dot2c_f32_bf16 v20, s7, v166\n\t"
    "v_dot2c_f32_bf16 v22, v166, v166\n\t"
    "buffer_load_dwordx4 v33, s[8:11], s63 offen offset:1024 lds\n\t"
    "v_dot2c_f32_bf16 v21, s7, v167\n\t"
    "v_dot2c_f32_bf16 v23, v167, v167\n\t"
    "s_waitcnt lgkmcnt(5)\n\t"
    "v_dot2c_f32_bf16 v20, s7, v168\n\t"
    "v_dot2c_f32_bf16 v22, v168, v168\n\t"
    "v_dot2c_f32_bf16 v21, s7, v169\n\t"
    "v_dot2c_f32_bf16 v23, v169, v169\n\t"
    "v_dot2c_f32_bf16 v20, s7, v170\n\t"
    "v_dot2c_f32_bf16 v22, v170, v170\n\t"
    "v_dot2c_f32_bf16 v21, s7, v171\n\t"
    "v_dot2c_f32_bf16 v23, v171, v171\n\t"
    "s_waitcnt lgkmcnt(4)\n\t"
    "v_dot2c_f32_bf16 v20, s7, v172\n\t"
    "v_dot2c_f32_bf16 v22, v172, v172\n\t"
    "v_dot2c_f32_bf16 v21, s7, v173\n\t"
    "v_dot2c_f32_bf16 v23, v173, v173\n\t"
    "buffer_load_dwordx4 v34, s[8:11], s63 offen offset:2048 lds\n\t"
    "v_dot2c_f32_bf16 v20, s7, v174\n\t"
    "v_dot2c_f32_bf16 v22, v174, v174\n\t"
    "v_dot2c_f32_bf16 v21, s7, v175\n\t"
    "v_dot2c_f32_bf16 v23, v175, v175\n\t"
    "v_fma_f32 v108, -v107, v107, v108\n\t"
    "v_add_f32 v109, v109, v109\n\t"
    "v_mul_f32 v110, v108, v108\n\t"
    "v_mul_f32 v111, v108, v109\n\t"
    "v_fma_f32 v110, -v109, v109, v110\n\t"
    "v_add_f32 v111, v111, v111\n\t"
    "v_accvgpr_write_b32 a36, v98\n\t"
    "v_accvgpr_write_b32 a37, v99\n\t"
    "v_accvgpr_write_b32 a42, v110\n\t"
    "v_accvgpr_write_b32 a43, v111\n\t"
    "buffer_load_dwordx4 v35, s[8:11], s63 offen offset:3072 lds\n\t"
    "v_and_b32 v86, 1, v72\n\t"
    "v_cmp_ne_u32 vcc, 0, v86\n\t"
    "s_nop 1\n\t"
    "v_cndmask_b32 v98, 1.0, v98, vcc\n\t"
    "v_cndmask_b32 v99, 0, v99, vcc\n\t"
    "v_cndmask_b32 v104, 1.0, v104, vcc\n\t"
    "v_cndmask_b32 v105, 0, v105, vcc\n\t"
    "v_and_b32 v86, 2, v72\n\t"
    "v_cmp_ne_u32 vcc, 0, v86\n\t"
    "s_nop 1\n\t"
    "v_cndmask_b32 v100, 1.0, v100, vcc\n\t"
    "v_cndmask_b32 v101, 0, v101, vcc\n\t"
    "v_cndmask_b32 v106, 1.0, v106, vcc\n\t"
    "v_cndmask_b32 v107, 0, v107, vcc\n\t"
    "v_and_b32 v86, 4, v72\n\t"
    "v_cmp_ne_u32 vcc, 0, v86\n\t"
    "s_nop 1\n\t"
    "v_cndmask_b32 v102, 1.0, v102, vcc\n\t"
    "s_waitcnt vmcnt(12)\n\t"
    "s_cmp_lg_u32 s31, s60\n\t"
    "s_cbranch_scc1 Lxs4_%=\n\t"
    "ds_read_b128 a[220:223], v42 offset:16384\n\t"
    "ds_read_b128 a[224:227], v43 offset:16384\n\t"
    "Lxs4_%=:\n\t"
    "ds_read_b128 v[192:195], v36 offset:16384\n\t"
    "ds_read_b128 v[196:199], v37 offset:16384\n\t"
    "ds_read_b128 v[200:203], v38 offset:16384\n\t"
    "ds_read_b128 v[204:207], v39 offset:16384\n\t"
    "s_mov_b32 m0, s52\n\t"
    "s_nop 0\n\t"
    "buffer_load_dwordx4 v5, s[20:23], s64 offen lds\n\t"
    "s_waitcnt lgkmcnt(7)\n\t"
    "v_dot2c_f32_bf16 v20, s7, v176\n\t"
    "v_dot2c_f32_bf16 v22, v176, v176\n\t"
    "v_dot2c_f32_bf16 v21, s7, v177\n\t"
    "v_dot2c_f32_bf16 v23, v177, v177\n\t"
    "v_dot2c_f32_bf16 v20, s7, v178\n\t"
    "v_dot2c_f32_bf16 v22, v178, v178\n\t"
    "v_dot2c_f32_bf16 v21, s7, v179\n\t"
    "v_dot2c_f32_bf16 v23, v179, v179\n\t"
    "s_waitcnt lgkmcnt(6)\n\t"
    "v_dot2c_f32_bf16 v20, s7, v180\n\t"
    "v_dot2c_f32_bf16 v22, v180, v180\n\t"
    "v_dot2c_f32_bf16 v21, s7, v181\n\t"
    "v_dot2c_f32_bf16 v23, v181, v181\n\t"
    "v_dot2c_f32_bf16 v20, s7, v182\n\t"
    "buffer_load_dwordx4 v6, s[20:23], s64 offen offset:1024 lds\n\t"
    "v_dot2c_f32_bf16 v22, v182, v182\n\t"
    "v_dot2c_f32_bf16 v21, s7, v183\n\t"
    "v_dot2c_f32_bf16 v23, v183, v183\n\t"
    "s_waitcnt lgkmcnt(5)\n\t"
    "v_dot2c_f32_bf16 v20, s7, v184\n\t"
    "v_dot2c_f32_bf16 v22, v184, v184\n\t"
    "v_dot2c_f32_bf16 v21, s7, v185\n\t"
    "v_dot2c_f32_bf16 v23, v185, v185\n\t"
    "v_dot2c_f32_bf16 v20, s7, v186\n\t"
    "v_dot2c_f32_bf16 v22, v186, v186\n\t"
    "v_dot2c_f32_bf16 v21, s7, v187\n\t"
    "v_dot2c_f32_bf16 v23, v187, v187\n\t"
    "s_waitcnt lgkmcnt(4)\n\t"
    "v_dot2c_f32_bf16 v20, s7, v188\n\t"
    "v_dot2c_f32_bf16 v22, v188, v188\n\t"
    "buffer_load_dwordx4 v5, s[24:27], s64 offen offset:2048 lds\n\t"
    "v_dot2c_f32_bf16 v21, s7, v189\n\t"
    "v_dot2c_f32_bf16 v23, v189, v189\n\t"
    "v_dot2c_f32_bf16 v20, s7, v190\n\t"
    "v_dot2c_f32_bf16 v22, v190, v190\n\t"
    "v_dot2c_f32_bf16 v21, s7, v191\n\t"
    "v_dot2c_f32_bf16 v23, v191, v191\n\t"
    "v_cndmask_b32 v103, 0, v103, vcc\n\t"
    "v_cndmask_b32 v108, 1.0, v108, vcc\n\t"
    "v_cndmask_b32 v109, 0, v109, vcc\n\t"
    "v_mul_f32 v90, v98, v100\n\t"
    "v_mul_f32 v91, v98, v101\n\t"
    "v_fma_f32 v90, -v99, v101, v90\n\t"
    "v_fmac_f32 v91, v99, v100\n\t"
    "buffer_load_dwordx4 v6, s[24:27], s64 offen offset:3072 lds\n\t"
    "v_mul_f32 v92, v90, v102\n\t"
    "v_mul_f32 v93, v90, v103\n\t"
    "v_fma_f32 v92, -v91, v103, v92\n\t"
    "v_fmac_f32 v93, v91, v102\n\t"
    "v_accvgpr_write_b32 a38, v92\n\t"
    "v_accvgpr_write_b32 a39, v93\n\t"
    "v_mul_f32 v90, v104, v106\n\t"
    "v_mul_f32 v91, v104, v107\n\t"
    "v_fma_f32 v90, -v105, v107, v90\n\t"
    "v_fmac_f32 v91, v105, v106\n\t"
    "v_mul_f32 v92, v90, v108\n\t"
    "v_mul_f32 v93, v90, v109\n\t"
    "v_fma_f32 v92, -v91, v109, v92\n\t"
    "v_fmac_f32 v93, v91, v108\n\t"
    "v_accvgpr_write_b32 a40, v92\n\t"
    "v_accvgpr_write_b32 a41, v93\n\t"
    "s_waitcnt vmcnt(12)\n\t"
    "s_cmp_lg_u32 s31, s61\n\t"
    "s_cbranch_scc1 Lxs5_%=\n\t"
    "ds_read_b128 a[220:223], v42 offset:20480\n\t"
    "ds_read_b128 a[224:227], v43 offset:20480\n\t"
    "Lxs5_%=:\n\t"
    "ds_read_b128 v[208:211], v36 offset:20480\n\t"
    "ds_read_b128 v[212:215], v37 offset:20480\n\t"
    "ds_read_b128 v[216:219], v38 offset:20480\n\t"
    "ds_read_b128 v[220:223], v39 offset:20480\n\t"
    "s_add_u32 m0, s52, 4096\n\t"
    "s_nop 0\n\t"
    "buffer_load_dwordx4 v4, s[12:15], s56 offen lds\n\t"
    "s_waitcnt lgkmcnt(7)\n\t"
    "v_dot2c_f32_bf16 v20, s7, v192\n\t"
    "v_dot2c_f32_bf16 v22, v192, v192\n\t"
    "v_dot2c_f32_bf16 v21, s7, v193\n\t"
    "v_dot2c_f32_bf16 v23, v193, v193\n\t"
    "v_dot2c_f32_bf16 v20, s7, v194\n\t"
    "v_dot2c_f32_bf16 v22, v194, v194\n\t"
    "v_dot2c_f32_bf16 v21, s7, v195\n\t"
    "v_dot2c_f32_bf16 v23, v195, v195\n\t"
    "buffer_load_dwordx4 v4, s[16:19], s56 offen offset:1024 lds\n\t"
    "s_waitcnt lgkmcnt(6)\n\t"
    "v_dot2c_f32_bf16 v20, s7, v196\n\t"
    "v_dot2c_f32_bf16 v22, v196, v196\n\t"
    "v_dot2c_f32_bf16 v21, s7, v197\n\t"
    "v_dot2c_f32_bf16 v23, v197, v197\n\t"
    "v_dot2c_f32_bf16 v20, s7, v198\n\t"
    "v_dot2c_f32_bf16 v22, v198, v198\n\t"
    "v_dot2c_f32_bf16 v21, s7, v199\n\t"
    "v_dot2c_f32_bf16 v23, v199, v199\n\t"
    "s_add_u32 m0, s52, 6144\n\t"
    "s_nop 0\n\t"
    "buffer_load_dwordx4 v5, s[20:23], s65 offen lds\n\t"
    "s_waitcnt lgkmcnt(5)\n\t"
    "v_dot2c_f32_bf16 v20, s7, v200\n\t"
    "v_dot2c_f32_bf16 v22, v200, v200\n\t"
    "v_dot2c_f32_bf16 v21, s7, v201\n\t"
    "v_dot2c_f32_bf16 v23, v201, v201\n\t"
    "v_dot2c_f32_bf16 v20, s7, v202\n\t"
    "v_dot2c_f32_bf16 v22, v202, v202\n\t"
    "v_dot2c_f32_bf16 v21, s7, v203\n\t"
    "v_dot2c_f32_bf16 v23, v203, v203\n\t"
    "buffer_load_dwordx4 v6, s[20:23], s65 offen offset:1024 lds\n\t"
    "s_waitcnt lgkmcnt(4)\n\t"
    "v_dot2c_f32_bf16 v20, s7, v204\n\t"
    "v_dot2c_f32_bf16 v22, v204, v204\n\t"
    "v_dot2c_f32_bf16 v21, s7, v205\n\t"
    "v_dot2c_f32_bf16 v23, v205, v205\n\t"
    "v_dot2c_f32_bf16 v20, s7, v206\n\t"
    "v_dot2c_f32_bf16 v22, v206, v206\n\t"
    "v_dot2c_f32_bf16 v21, s7, v207\n\t"
    "v_dot2c_f32_bf16 v23, v207, v207\n\t"
    "s_mov_b64 exec, 0\n\t"
    "s_cmp_eq_u32 s95, 0\n\t"
    "s_cbranch_scc1 Lpad0_%=\n\t"
    "s_cmp_eq_u32 s95, 1\n\t"
    "s_cbranch_scc1 Lpad1_%=\n\t"
    "s_cmp_eq_u32 s95, 2\n\t"
    "s_cbranch_scc1 Lpad2_%=\n\t"
    "s_branch Lpad3_%=\n\t"
    "Lpret_%=:\n\t"
    "s_mov_b64 exec, -1\n\t"
    "s_waitcnt vmcnt(12)\n\t"
    "s_cmp_lg_u32 s31, s62\n\t"
    "s_cbranch_scc1 Lxs6_%=\n\t"
    "ds_read_b128 a[220:223], v42 offset:24576\n\t"
    "ds_read_b128 a[224:227], v43 offset:24576\n\t"
    "Lxs6_%=:\n\t"
    "ds_read_b128 v[224:227], v36 offset:24576\n\t"
    "ds_read_b128 v[228:231], v37 offset:24576\n\t"
    "ds_read_b128 v[232:235], v38 offset:24576\n\t"
    "ds_read_b128 v[236:239], v39 offset:24576\n\t"
    "buffer_load_dwordx4 v5, s[24:27], s65 offen offset:2048 lds\n\t"
    "s_waitcnt lgkmcnt(7)\n\t"
    "v_dot2c_f32_bf16 v20, s7, v208\n\t"
    "v_dot2c_f32_bf16 v22, v208, v208\n\t"
    "v_dot2c_f32_bf16 v21, s7, v209\n\t"
    "v_dot2c_f32_bf16 v23, v209, v209\n\t"
    "v_dot2c_f32_bf16 v20, s7, v210\n\t"
    "v_dot2c_f32_bf16 v22, v210, v210\n\t"
    "v_dot2c_f32_bf16 v21, s7, v211\n\t"
    "v_dot2c_f32_bf16 v23, v211, v211\n\t"
    "buffer_load_dwordx4 v6, s[24:27], s65 offen offset:3072 lds\n\t"
    "s_waitcnt lgkmcnt(6)\n\t"
    "v_dot2c_f32_bf16 v20, s7, v212\n\t"
    "v_dot2c_f32_bf16 v22, v212, v212\n\t"
    "v_dot2c_f32_bf16 v21, s7, v213\n\t"
    "v_dot2c_f32_bf16 v23, v213, v213\n\t"
    "v_dot2c_f32_bf16 v20, s7, v214\n\t"
    "v_dot2c_f32_bf16 v22, v214, v214\n\t"
    "v_dot2c_f32_bf16 v21, s7, v215\n\t"
    "v_dot2c_f32_bf16 v23, v215, v215\n\t"
    "s_add_u32 m0, s52, 10240\n\t"
    "s_nop 0\n\t"
    "buffer_load_dwordx4 v4, s[12:15], s57 offen lds\n\t"
    "s_waitcnt lgkmcnt(5)\n\t"
    "v_dot2c_f32_bf16 v20, s7, v216\n\t"
    "v_dot2c_f32_bf16 v22, v216, v216\n\t"
    "v_dot2c_f32_bf16 v21, s7, v217\n\t"
    "v_dot2c_f32_bf16 v23, v217, v217\n\t"
    "v_dot2c_f32_bf16 v20, s7, v218\n\t"
    "v_dot2c_f32_bf16 v22, v218, v218\n\t"
    "v_dot2c_f32_bf16 v21, s7, v219\n\t"
    "v_dot2c_f32_bf16 v23, v219, v219\n\t"
    "buffer_load_dwordx4 v4, s[16:19], s57 offen offset:1024 lds\n\t"
    "s_waitcnt lgkmcnt(4)\n\t"
    "v_dot2c_f32_bf16 v20, s7, v220\n\t"
    "v_dot2c_f32_bf16 v22, v220, v220\n\t"
    "Lpad0_%=:\n\t"
    "s_cbranch_execz Lpad4_%=\n\t"
    "v_dot2c_f32_bf16 v21, s7, v221\n\t"
    "v_dot2c_f32_bf16 v23, v221, v221\n\t"
    "v_dot2c_f32_bf16 v20, s7, v222\n\t"
    "v_dot2c_f32_bf16 v22, v222, v222\n\t"
    "v_dot2c_f32_bf16 v21, s7, v223\n\t"
    "v_dot2c_f32_bf16 v23, v223, v223\n\t"
    "s_waitcnt vmcnt(12)\n\t"
    "s_cmp_lg_u32 s31, s63\n\t"
    "s_cbranch_scc1 Lxs7_%=\n\t"
    "ds_read_b128 a[220:223], v42 offset:28672\n\t"
    "ds_read_b128 a[224:227], v43 offset:28672\n\t"
    "Lxs7_%=:\n\t"
    "ds_read_b128 v[240:243], v36 offset:28672\n\t"
    "ds_read_b128 v[244:247], v37 offset:28672\n\t"
    "ds_read_b128 v[248:251], v38 offset:28672\n\t"
    "ds_read_b128 v[252:255], v39 offset:28672\n\t"
    "s_add_u32 m0, s52, 12288\n\t"
    "s_nop 0\n\t"
    "buffer_load_dwordx4 v5, s[20:23], s66 offen lds\n\t"
    "s_waitcnt lgkmcnt(7)\n\t"
    "v_dot2c_f32_bf16 v20, s7, v224\n\t"
    "v_dot2c_f32_bf16 v22, v224, v224\n\t"
    "v_dot2c_f32_bf16 v21, s7, v225\n\t"
    "v_dot2c_f32_bf16 v23, v225, v225\n\t"
    "v_dot2c_f32_bf16 v20, s7, v226\n\t"
    "v_dot2c_f32_bf16 v22, v226, v226\n\t"
    "v_dot2c_f32_bf16 v21, s7, v227\n\t"
    "v_dot2c_f32_bf16 v23, v227, v227\n\t"
    "s_waitcnt lgkmcnt(6)\n\t"
    "v_dot2c_f32_bf16 v20, s7, v228\n\t"
    "v_dot2c_f32_bf16 v22, v228, v228\n\t"
    "v_dot2c_f32_bf16 v21, s7, v229\n\t"
    "v_dot2c_f32_bf16 v23, v229, v229\n\t"
    "v_dot2c_f32_bf16 v20, s7, v230\n\t"
    "v_dot2c_f32_bf16 v22, v230, v230\n\t"
    "v_dot2c_f32_bf16 v21, s7, v231\n\t"
    "v_dot2c_f32_bf16 v23, v231, v231\n\t"
    "buffer_load_dwordx4 v6, s[20:23], s66 offen offset:1024 lds\n\t"
    "s_waitcnt lgkmcnt(5)\n\t"
    "v_dot2c_f32_bf16 v20, s7, v232\n\t"
    "v_dot2c_f32_bf16 v22, v232, v232\n\t"
    "v_dot2c_f32_bf16 v21, s7, v233\n\t"
    "v_dot2c_f32_bf16 v23, v233, v233\n\t"
    "v_dot2c_f32_bf16 v20, s7, v234\n\t"
    "v_dot2c_f32_bf16 v22, v234, v234\n\t"
    "v_dot2c_f32_bf16 v21, s7, v235\n\t"
    "v_dot2c_f32_bf16 v23, v235, v235\n\t"
    "s_waitcnt lgkmcnt(4)\n\t"
    "v_dot2c_f32_bf16 v20, s7, v236\n\t"
    "v_dot2c_f32_bf16 v22, v236, v236\n\t"
    "v_dot2c_f32_bf16 v21, s7, v237\n\t"
    "v_dot2c_f32_bf16 v23, v237, v237\n\t"
    "v_dot2c_f32_bf16 v20, s7, v238\n\t"
    "v_dot2c_f32_bf16 v22, v238, v238\n\t"
    "v_dot2c_f32_bf16 v21, s7, v239\n\t"
    "v_dot2c_f32_bf16 v23, v239, v239\n\t"
    "s_waitcnt lgkmcnt(3)\n\t"
    "v_dot2c_f32_bf16 v20, s7, v240\n\t"
    "v_dot2c_f32_bf16 v22, v240, v240\n\t"
    "v_dot2c_f32_bf16 v21, s7, v241\n\t"
    "v_dot2c_f32_bf16 v23, v241, v241\n\t"
    "v_dot2c_f32_bf16 v20, s7, v242\n\t"
    "v_dot2c_f32_bf16 v22, v242, v242\n\t"
    "v_dot2c_f32_bf16 v21, s7, v243\n\t"
    "v_dot2c_f32_bf16 v23, v243, v243\n\t"
    "s_waitcnt lgkmcnt(2)\n\t"
    "v_dot2c_f32_bf16 v20, s7, v244\n\t"
    "v_dot2c_f32_bf16 v22, v244, v244\n\t"
    "v_dot2c_f32_bf16 v21, s7, v245\n\t"
    "v_dot2c_f32_bf16 v23, v245, v245\n\t"
    "v_dot2c_f32_bf16 v20, s7, v246\n\t"
    "v_dot2c_f32_bf16 v22, v246, v246\n\t"
    "v_dot2c_f32_bf16 v21, s7, v247\n\t"
    "v_dot2c_f32_bf16 v23, v247, v247\n\t"
    "s_waitcnt lgkmcnt(1)\n\t"
    "v_dot2c_f32_bf16 v20, s7, v248\n\t"
    "v_dot2c_f32_bf16 v22, v248, v248\n\t"
    "v_dot2c_f32_bf16 v21, s7, v249\n\t"
    "v_dot2c_f32_bf16 v23, v249, v249\n\t"
    "v_dot2c_f32_bf16 v20, s7, v250\n\t"
    "v_dot2c_f32_bf16 v22, v250, v250\n\t"
    "v_dot2c_f32_bf16 v21, s7, v251\n\t"
    "v_dot2c_f32_bf16 v23, v251, v251\n\t"
    "s_waitcnt lgkmcnt(0)\n\t"
    "v_dot2c_f32_bf16 v20, s7, v252\n\t"
    "v_dot2c_f32_bf16 v22, v252, v252\n\t"
    "v_dot2c_f32_bf16 v21, s7, v253\n\t"
    "v_dot2c_f32_bf16 v23, v253, v253\n\t"
    "v_dot2c_f32_bf16 v20, s7, v254\n\t"
    "v_dot2c_f32_bf16 v22, v254, v254\n\t"
    "v_dot2c_f32_bf16 v21, s7, v255\n\t"
    "v_dot2c_f32_bf16 v23, v255, v255\n\t"
    "s_nop 1\n\t"
    "v_add_f32 v20, v20, v21\n\t"
    "v_add_f32 v22, v22, v23\n\t"
    "v_mov_b32 v21, v20\n\t"
    "v_mov_b32 v23, v22\n\t"
    "s_nop 0\n\t"
    "v_permlane32_swap_b32 v20, v21\n\t"
    "v_permlane32_swap_b32 v22, v23\n\t"
    "v_add_f32 v20, v20, v21\n\t"
    "v_add_f32 v22, v22, v23\n\t"
    "v_mul_f32 v20, 0x3b000000, v20\n\t"
    "v_mul_f32 v22, 0x3b000000, v22\n\t"
    "v_fma_f32 v22, -v20, v20, v22\n\t"
    "v_max_f32 v22, 0, v22\n\t"
    "v_cvt_pk_bf16_f32 v24, v22, v20\n\t"
    "v_lshlrev_b32 v22, 16, v24\n\t"
    "v_and_b32 v20, s6, v24\n\t"
    "v_add_f32 v22, 0x3727c5ac, v22\n\t"
    "v_cvt_pk_bf16_f32 v22, v22, v22\n\t"
    "v_lshlrev_b32 v22, 16, v22\n\t"
    "v_sqrt_f32 v22, v22\n\t"
    "s_nop 0\n\t"
    "v_cvt_pk_bf16_f32 v22, v22, v22\n\t"
    "v_lshlrev_b32 v22, 16, v22\n\t"
    "v_rcp_f32 v14, v22\n\t"
    "s_nop 0\n\t"
    "v_mul_f32 v15, -v20, v14\n\t"
    "v_lshlrev_b32 v20, 16, v128\n\t"
    "v_and_b32 v21, s6, v128\n\t"
    "v_pk_fma_f32 v[20:21], v[20:21], v[14:15], v[14:15] op_sel:[0,0,1] op_sel_hi:[1,0,1]\n\t"
    "v_cvt_pk_bf16_f32 v128, v20, v21\n\t"
    "v_lshlrev_b32 v22, 16, v129\n\t"
    "v_and_b32 v23, s6, v129\n\t"
    "v_pk_fma_f32 v[22:23], v[22:23], v[14:15], v[14:15] op_sel:[0,0,1] op_sel_hi:[1,0,1]\n\t"
    "v_cvt_pk_bf16_f32 v129, v22, v23\n\t"
    "v_lshlrev_b32 v20, 16, v130\n\t"
    "v_and_b32 v21, s6, v130\n\t"
    "v_pk_fma_f32 v[20:21], v[20:21], v[14:15], v[14:15] op_sel:[0,0,1] op_sel_hi:[1,0,1]\n\t"
    "v_cvt_pk_bf16_f32 v130, v20, v21\n\t"
    "v_lshlrev_b32 v22, 16, v131\n\t"
    "v_and_b32 v23, s6, v131\n\t"
    "v_pk_fma_f32 v[22:23], v[22:23], v[14:15], v[14:15] op_sel:[0,0,1] op_sel_hi:[1,0,1]\n\t"
    "v_cvt_pk_bf16_f32 v131, v22, v23\n\t"
    "v_lshlrev_b32 v20, 16, v132\n\t"
    "v_and_b32 v21, s6, v132\n\t"
    "v_pk_fma_f32 v[20:21], v[20:21], v[14:15], v[14:15] op_sel:[0,0,1] op_sel_hi:[1,0,1]\n\t"
    "v_cvt_pk_bf16_f32 v132, v20, v21\n\t"
    "v_lshlrev_b32 v22, 16, v133\n\t"
    "v_and_b32 v23, s6, v133\n\t"
    "v_pk_fma_f32 v[22:23], v[22:23], v[14:15], v[14:15] op_sel:[0,0,1] op_sel_hi:[1,0,1]\n\t"
    "v_cvt_pk_bf16_f32 v133, v22, v23\n\t"
    "v_lshlrev_b32 v20, 16, v134\n\t"
    "v_and_b32 v21, s6, v134\n\t"
    "v_pk_fma_f32 v[20:21], v[20:21], v[14:15], v[14:15] op_sel:[0,0,1] op_sel_hi:[1,0,1]\n\t"
    "v_cvt_pk_bf16_f32 v134, v20, v21\n\t"
    "v_lshlrev_b32 v22, 16, v135\n\t"
    "v_and_b32 v23, s6, v135\n\t"
    "v_pk_fma_f32 v[22:23], v[22:23], v[14:15], v[14:15] op_sel:[0,0,1] op_sel_hi:[1,0,1]\n\t"
    "v_cvt_pk_bf16_f32 v135, v22, v23\n\t"
    "v_lshlrev_b32 v20, 16, v136\n\t"
    "v_and_b32 v21, s6, v136\n\t"
    "v_pk_fma_f32 v[20:21], v[20:21], v[14:15], v[14:15] op_sel:[0,0,1] op_sel_hi:[1,0,1]\n\t"
    "v_cvt_pk_bf16_f32 v136, v20, v21\n\t"
    "v_lshlrev_b32 v22, 16, v137\n\t"
    "v_and_b32 v23, s6, v137\n\t"
    "v_pk_fma_f32 v[22:23], v[22:23], v[14:15], v[14:15] op_sel:[0,0,1] op_sel_hi:[1,0,1]\n\t"
    "v_cvt_pk_bf16_f32 v137, v22, v23\n\t"
    "v_lshlrev_b32 v20, 16, v138\n\t"
    "v_and_b32 v21, s6, v138\n\t"
    "v_pk_fma_f32 v[20:21], v[20:21], v[14:15], v[14:15] op_sel:[0,0,1] op_sel_hi:[1,0,1]\n\t"
    "v_cvt_pk_bf16_f32 v138, v20, v21\n\t"
    "v_lshlrev_b32 v22, 16, v139\n\t"
    "v_and_b32 v23, s6, v139\n\t"
    "v_pk_fma_f32 v[22:23], v[22:23], v[14:15], v[14:15] op_sel:[0,0,1] op_sel_hi:[1,0,1]\n\t"
    "v_cvt_pk_bf16_f32 v139, v22, v23\n\t"
    "v_lshlrev_b32 v20, 16, v140\n\t"
    "v_and_b32 v21, s6, v140\n\t"
    "v_pk_fma_f32 v[20:21], v[20:21], v[14:15], v[14:15] op_sel:[0,0,1] op_sel_hi:[1,0,1]\n\t"
    "v_cvt_pk_bf16_f32 v140, v20, v21\n\t"
    "v_lshlrev_b32 v22, 16, v141\n\t"
    "v_and_b32 v23, s6, v141\n\t"
    "v_pk_fma_f32 v[22:23], v[22:23], v[14:15], v[14:15] op_sel:[0,0,1] op_sel_hi:[1,0,1]\n\t"
    "v_cvt_pk_bf16_f32 v141, v22, v23\n\t"
    "v_lshlrev_b32 v20, 16, v142\n\t"
    "v_and_b32 v21, s6, v142\n\t"
    "v_pk_fma_f32 v[20:21], v[20:21], v[14:15], v[14:15] op_sel:[0,0,1] op_sel_hi:[1,0,1]\n\t"
    "v_cvt_pk_bf16_f32 v142, v20, v21\n\t"
    "v_lshlrev_b32 v22, 16, v143\n\t"
    "v_and_b32 v23, s6, v143\n\t"
    "v_pk_fma_f32 v[22:23], v[22:23], v[14:15], v[14:15] op_sel:[0,0,1] op_sel_hi:[1,0,1]\n\t"
    "v_cvt_pk_bf16_f32 v143, v22, v23\n\t"
    "v_lshlrev_b32 v20, 16, v144\n\t"
    "v_and_b32 v21, s6, v144\n\t"
    "v_pk_fma_f32 v[20:21], v[20:21], v[14:15], v[14:15] op_sel:[0,0,1] op_sel_hi:[1,0,1]\n\t"
    "v_cvt_pk_bf16_f32 v144, v20, v21\n\t"
    "v_lshlrev_b32 v22, 16, v145\n\t"
    "v_and_b32 v23, s6, v145\n\t"
    "v_pk_fma_f32 v[22:23], v[22:23], v[14:15], v[14:15] op_sel:[0,0,1] op_sel_hi:[1,0,1]\n\t"
    "v_cvt_pk_bf16_f32 v145, v22, v23\n\t"
    "v_lshlrev_b32 v20, 16, v146\n\t"
    "v_and_b32 v21, s6, v146\n\t"
    "v_pk_fma_f32 v[20:21], v[20:21], v[14:15], v[14:15] op_sel:[0,0,1] op_sel_hi:[1,0,1]\n\t"
    "v_cvt_pk_bf16_f32 v146, v20, v21\n\t"
    "v_lshlrev_b32 v22, 16, v147\n\t"
    "v_and_b32 v23, s6, v147\n\t"
    "v_pk_fma_f32 v[22:23], v[22:23], v[14:15], v[14:15] op_sel:[0,0,1] op_sel_hi:[1,0,1]\n\t"
    "v_cvt_pk_bf16_f32 v147, v22, v23\n\t"
    "v_lshlrev_b32 v20, 16, v148\n\t"
    "v_and_b32 v21, s6, v148\n\t"
    "v_pk_fma_f32 v[20:21], v[20:21], v[14:15], v[14:15] op_sel:[0,0,1] op_sel_hi:[1,0,1]\n\t"
    "v_cvt_pk_bf16_f32 v148, v20, v21\n\t"
    "v_lshlrev_b32 v22, 16, v149\n\t"
    "v_and_b32 v23, s6, v149\n\t"
    "v_pk_fma_f32 v[22:23], v[22:23], v[14:15], v[14:15] op_sel:[0,0,1] op_sel_hi:[1,0,1]\n\t"
    "v_cvt_pk_bf16_f32 v149, v22, v23\n\t"
    "v_lshlrev_b32 v20, 16, v150\n\t"
    "v_and_b32 v21, s6, v150\n\t"
    "v_pk_fma_f32 v[20:21], v[20:21], v[14:15], v[14:15] op_sel:[0,0,1] op_sel_hi:[1,0,1]\n\t"
    "v_cvt_pk_bf16_f32 v150, v20, v21\n\t"
    "v_lshlrev_b32 v22, 16, v151\n\t"
    "v_and_b32 v23, s6, v151\n\t"
    "v_pk_fma_f32 v[22:23], v[22:23], v[14:15], v[14:15] op_sel:[0,0,1] op_sel_hi:[1,0,1]\n\t"
    "v_cvt_pk_bf16_f32 v151, v22, v23\n\t"
    "v_lshlrev_b32 v20, 16, v152\n\t"
    "v_and_b32 v21, s6, v152\n\t"
    "v_pk_fma_f32 v[20:21], v[20:21], v[14:15], v[14:15] op_sel:[0,0,1] op_sel_hi:[1,0,1]\n\t"
    "v_cvt_pk_bf16_f32 v152, v20, v21\n\t"
    "v_lshlrev_b32 v22, 16, v153\n\t"
    "v_and_b32 v23, s6, v153\n\t"
    "v_pk_fma_f32 v[22:23], v[22:23], v[14:15], v[14:15] op_sel:[0,0,1] op_sel_hi:[1,0,1]\n\t"
    "v_cvt_pk_bf16_f32 v153, v22, v23\n\t"
    "v_lshlrev_b32 v20, 16, v154\n\t"
    "v_and_b32 v21, s6, v154\n\t"
    "v_pk_fma_f32 v[20:21], v[20:21], v[14:15], v[14:15] op_sel:[0,0,1] op_sel_hi:[1,0,1]\n\t"
    "v_cvt_pk_bf16_f32 v154, v20, v21\n\t"
    "v_lshlrev_b32 v22, 16, v155\n\t"
    "v_and_b32 v23, s6, v155\n\t"
    "v_pk_fma_f32 v[22:23], v[22:23], v[14:15], v[14:15] op_sel:[0,0,1] op_sel_hi:[1,0,1]\n\t"
    "v_cvt_pk_bf16_f32 v155, v22, v23\n\t"
    "v_lshlrev_b32 v20, 16, v156\n\t"
    "v_and_b32 v21, s6, v156\n\t"
    "v_pk_fma_f32 v[20:21], v[20:21], v[14:15], v[14:15] op_sel:[0,0,1] op_sel_hi:[1,0,1]\n\t"
    "v_cvt_pk_bf16_f32 v156, v20, v21\n\t"
    "v_lshlrev_b32 v22, 16, v157\n\t"
    "v_and_b32 v23, s6, v157\n\t"
    "v_pk_fma_f32 v[22:23], v[22:23], v[14:15], v[14:15] op_sel:[0,0,1] op_sel_hi:[1,0,1]\n\t"
    "v_cvt_pk_bf16_f32 v157, v22, v23\n\t"
    "v_lshlrev_b32 v20, 16, v158\n\t"
    "v_and_b32 v21, s6, v158\n\t"
    "v_pk_fma_f32 v[20:21], v[20:21], v[14:15], v[14:15] op_sel:[0,0,1] op_sel_hi:[1,0,1]\n\t"
    "v_cvt_pk_bf16_f32 v158, v20, v21\n\t"
    "v_lshlrev_b32 v22, 16, v159\n\t"
    "v_and_b32 v23, s6, v159\n\t"
    "v_pk_fma_f32 v[22:23], v[22:23], v[14:15], v[14:15] op_sel:[0,0,1] op_sel_hi:[1,0,1]\n\t"
    "v_cvt_pk_bf16_f32 v159, v22, v23\n\t"
    "v_lshlrev_b32 v20, 16, v160\n\t"
    "v_and_b32 v21, s6, v160\n\t"
    "v_pk_fma_f32 v[20:21], v[20:21], v[14:15], v[14:15] op_sel:[0,0,1] op_sel_hi:[1,0,1]\n\t"
    "v_cvt_pk_bf16_f32 v160, v20, v21\n\t"
    "v_lshlrev_b32 v22, 16, v161\n\t"
    "v_and_b32 v23, s6, v161\n\t"
    "v_pk_fma_f32 v[22:23], v[22:23], v[14:15], v[14:15] op_sel:[0,0,1] op_sel_hi:[1,0,1]\n\t"
    "v_cvt_pk_bf16_f32 v161, v22, v23\n\t"
    "v_lshlrev_b32 v20, 16, v162\n\t"
    "v_and_b32 v21, s6, v162\n\t"
    "v_pk_fma_f32 v[20:21], v[20:21], v[14:15], v[14:15] op_sel:[0,0,1] op_sel_hi:[1,0,1]\n\t"
    "v_cvt_pk_bf16_f32 v162, v20, v21\n\t"
    "v_lshlrev_b32 v22, 16, v163\n\t"
    "v_and_b32 v23, s6, v163\n\t"
    "v_pk_fma_f32 v[22:23], v[22:23], v[14:15], v[14:15] op_sel:[0,0,1] op_sel_hi:[1,0,1]\n\t"
    "v_cvt_pk_bf16_f32 v163, v22, v23\n\t"
    "v_lshlrev_b32 v20, 16, v164\n\t"
    "v_and_b32 v21, s6, v164\n\t"
    "v_pk_fma_f32 v[20:21], v[20:21], v[14:15], v[14:15] op_sel:[0,0,1] op_sel_hi:[1,0,1]\n\t"
    "v_cvt_pk_bf16_f32 v164, v20, v21\n\t"
    "v_lshlrev_b32 v22, 16, v165\n\t"
    "v_and_b32 v23, s6, v165\n\t"
    "v_pk_fma_f32 v[22:23], v[22:23], v[14:15], v[14:15] op_sel:[0,0,1] op_sel_hi:[1,0,1]\n\t"
    "v_cvt_pk_bf16_f32 v165, v22, v23\n\t"
    "v_lshlrev_b32 v20, 16, v166\n\t"
    "v_and_b32 v21, s6, v166\n\t"
    "v_pk_fma_f32 v[20:21], v[20:21], v[14:15], v[14:15] op_sel:[0,0,1] op_sel_hi:[1,0,1]\n\t"
    "v_cvt_pk_bf16_f32 v166, v20, v21\n\t"
    "v_lshlrev_b32 v22, 16, v167\n\t"
    "v_and_b32 v23, s6, v167\n\t"
    "v_pk_fma_f32 v[22:23], v[22:23], v[14:15], v[14:15] op_sel:[0,0,1] op_sel_hi:[1,0,1]\n\t"
    "v_cvt_pk_bf16_f32 v167, v22, v23\n\t"
    "v_lshlrev_b32 v20, 16, v168\n\t"
    "v_and_b32 v21, s6, v168\n\t"
    "v_pk_fma_f32 v[20:21], v[20:21], v[14:15], v[14:15] op_sel:[0,0,1] op_sel_hi:[1,0,1]\n\t"
    "v_cvt_pk_bf16_f32 v168, v20, v21\n\t"
    "v_lshlrev_b32 v22, 16, v169\n\t"
    "v_and_b32 v23, s6, v169\n\t"
    "v_pk_fma_f32 v[22:23], v[22:23], v[14:15], v[14:15] op_sel:[0,0,1] op_sel_hi:[1,0,1]\n\t"
    "v_cvt_pk_bf16_f32 v169, v22, v23\n\t"
    "v_lshlrev_b32 v20, 16, v170\n\t"
    "v_and_b32 v21, s6, v170\n\t"
    "v_pk_fma_f32 v[20:21], v[20:21], v[14:15], v[14:15] op_sel:[0,0,1] op_sel_hi:[1,0,1]\n\t"
    "v_cvt_pk_bf16_f32 v170, v20, v21\n\t"
    "v_lshlrev_b32 v22, 16, v171\n\t"
    "v_and_b32 v23, s6, v171\n\t"
    "v_pk_fma_f32 v[22:23], v[22:23], v[14:15], v[14:15] op_sel:[0,0,1] op_sel_hi:[1,0,1]\n\t"
    "v_cvt_pk_bf16_f32 v171, v22, v23\n\t"
    "v_lshlrev_b32 v20, 16, v172\n\t"
    "v_and_b32 v21, s6, v172\n\t"
    "v_pk_fma_f32 v[20:21], v[20:21], v[14:15], v[14:15] op_sel:[0,0,1] op_sel_hi:[1,0,1]\n\t"
    "v_cvt_pk_bf16_f32 v172, v20, v21\n\t"
    "v_lshlrev_b32 v22, 16, v173\n\t"
    "v_and_b32 v23, s6, v173\n\t"
    "v_pk_fma_f32 v[22:23], v[22:23], v[14:15], v[14:15] op_sel:[0,0,1] op_sel_hi:[1,0,1]\n\t"
    "v_cvt_pk_bf16_f32 v173, v22, v23\n\t"
    "v_lshlrev_b32 v20, 16, v174\n\t"
    "v_and_b32 v21, s6, v174\n\t"
    "v_pk_fma_f32 v[20:21], v[20:21], v[14:15], v[14:15] op_sel:[0,0,1] op_sel_hi:[1,0,1]\n\t"
    "v_cvt_pk_bf16_f32 v174, v20, v21\n\t"
    "v_lshlrev_b32 v22, 16, v175\n\t"
    "v_and_b32 v23, s6, v175\n\t"
    "v_pk_fma_f32 v[22:23], v[22:23], v[14:15], v[14:15] op_sel:[0,0,1] op_sel_hi:[1,0,1]\n\t"
    "v_cvt_pk_bf16_f32 v175, v22, v23\n\t"
    "v_lshlrev_b32 v20, 16, v176\n\t"
    "v_and_b32 v21, s6, v176\n\t"
    "v_pk_fma_f32 v[20:21], v[20:21], v[14:15], v[14:15] op_sel:[0,0,1] op_sel_hi:[1,0,1]\n\t"
    "v_cvt_pk_bf16_f32 v176, v20, v21\n\t"
    "v_lshlrev_b32 v22, 16, v177\n\t"
    "v_and_b32 v23, s6, v177\n\t"
    "v_pk_fma_f32 v[22:23], v[22:23], v[14:15], v[14:15] op_sel:[0,0,1] op_sel_hi:[1,0,1]\n\t"
    "v_cvt_pk_bf16_f32 v177, v22, v23\n\t"
    "v_lshlrev_b32 v20, 16, v178\n\t"
    "v_and_b32 v21, s6, v178\n\t"
    "v_pk_fma_f32 v[20:21], v[20:21], v[14:15], v[14:15] op_sel:[0,0,1] op_sel_hi:[1,0,1]\n\t"
    "v_cvt_pk_bf16_f32 v178, v20, v21\n\t"
    "v_lshlrev_b32 v22, 16, v179\n\t"
    "v_and_b32 v23, s6, v179\n\t"
    "v_pk_fma_f32 v[22:23], v[22:23], v[14:15], v[14:15] op_sel:[0,0,1] op_sel_hi:[1,0,1]\n\t"
    "v_cvt_pk_bf16_f32 v179, v22, v23\n\t"
    "v_lshlrev_b32 v20, 16, v180\n\t"
    "v_and_b32 v21, s6, v180\n\t"
    "v_pk_fma_f32 v[20:21], v[20:21], v[14:15], v[14:15] op_sel:[0,0,1] op_sel_hi:[1,0,1]\n\t"
    "v_cvt_pk_bf16_f32 v180, v20, v21\n\t"
    "v_lshlrev_b32 v22, 16, v181\n\t"
    "v_and_b32 v23, s6, v181\n\t"
    "v_pk_fma_f32 v[22:23], v[22:23], v[14:15], v[14:15] op_sel:[0,0,1] op_sel_hi:[1,0,1]\n\t"
    "v_cvt_pk_bf16_f32 v181, v22, v23\n\t"
    "v_lshlrev_b32 v20, 16, v182\n\t"
    "v_and_b32 v21, s6, v182\n\t"
    "v_pk_fma_f32 v[20:21], v[20:21], v[14:15], v[14:15] op_sel:[0,0,1] op_sel_hi:[1,0,1]\n\t"
    "v_cvt_pk_bf16_f32 v182, v20, v21\n\t"
    "v_lshlrev_b32 v22, 16, v183\n\t"
    "v_and_b32 v23, s6, v183\n\t"
    "v_pk_fma_f32 v[22:23], v[22:23], v[14:15], v[14:15] op_sel:[0,0,1] op_sel_hi:[1,0,1]\n\t"
    "v_cvt_pk_bf16_f32 v183, v22, v23\n\t"
    "v_lshlrev_b32 v20, 16, v184\n\t"
    "v_and_b32 v21, s6, v184\n\t"
    "v_pk_fma_f32 v[20:21], v[20:21], v[14:15], v[14:15] op_sel:[0,0,1] op_sel_hi:[1,0,1]\n\t"
    "v_cvt_pk_bf16_f32 v184, v20, v21\n\t"
    "v_lshlrev_b32 v22, 16, v185\n\t"
    "v_and_b32 v23, s6, v185\n\t"
    "v_pk_fma_f32 v[22:23], v[22:23], v[14:15], v[14:15] op_sel:[0,0,1] op_sel_hi:[1,0,1]\n\t"
    "v_cvt_pk_bf16_f32 v185, v22, v23\n\t"
    "v_lshlrev_b32 v20, 16, v186\n\t"
    "v_and_b32 v21, s6, v186\n\t"
    "v_pk_fma_f32 v[20:21], v[20:21], v[14:15], v[14:15] op_sel:[0,0,1] op_sel_hi:[1,0,1]\n\t"
    "v_cvt_pk_bf16_f32 v186, v20, v21\n\t"
    "v_lshlrev_b32 v22, 16, v187\n\t"
    "v_and_b32 v23, s6, v187\n\t"
    "v_pk_fma_f32 v[22:23], v[22:23], v[14:15], v[14:15] op_sel:[0,0,1] op_sel_hi:[1,0,1]\n\t"
    "v_cvt_pk_bf16_f32 v187, v22, v23\n\t"
    "v_lshlrev_b32 v20, 16, v188\n\t"
    "v_and_b32 v21, s6, v188\n\t"
    "v_pk_fma_f32 v[20:21], v[20:21], v[14:15], v[14:15] op_sel:[0,0,1] op_sel_hi:[1,0,1]\n\t"
    "v_cvt_pk_bf16_f32 v188, v20, v21\n\t"
    "v_lshlrev_b32 v22, 16, v189\n\t"
    "v_and_b32 v23, s6, v189\n\t"
    "v_pk_fma_f32 v[22:23], v[22:23], v[14:15], v[14:15] op_sel:[0,0,1] op_sel_hi:[1,0,1]\n\t"
    "v_cvt_pk_bf16_f32 v189, v22, v23\n\t"
    "v_lshlrev_b32 v20, 16, v190\n\t"
    "v_and_b32 v21, s6, v190\n\t"
    "v_pk_fma_f32 v[20:21], v[20:21], v[14:15], v[14:15] op_sel:[0,0,1] op_sel_hi:[1,0,1]\n\t"
    "v_cvt_pk_bf16_f32 v190, v20, v21\n\t"
    "v_lshlrev_b32 v22, 16, v191\n\t"
    "v_and_b32 v23, s6, v191\n\t"
    "v_pk_fma_f32 v[22:23], v[22:23], v[14:15], v[14:15] op_sel:[0,0,1] op_sel_hi:[1,0,1]\n\t"
    "v_cvt_pk_bf16_f32 v191, v22, v23\n\t"
    "v_lshlrev_b32 v20, 16, v192\n\t"
    "v_and_b32 v21, s6, v192\n\t"
    "v_pk_fma_f32 v[20:21], v[20:21], v[14:15], v[14:15] op_sel:[0,0,1] op_sel_hi:[1,0,1]\n\t"
    "v_cvt_pk_bf16_f32 v192, v20, v21\n\t"
    "v_lshlrev_b32 v22, 16, v193\n\t"
    "v_and_b32 v23, s6, v193\n\t"
    "v_pk_fma_f32 v[22:23], v[22:23], v[14:15], v[14:15] op_sel:[0,0,1] op_sel_hi:[1,0,1]\n\t"
    "v_cvt_pk_bf16_f32 v193, v22, v23\n\t"
    "v_lshlrev_b32 v20, 16, v194\n\t"
    "v_and_b32 v21, s6, v194\n\t"
    "v_pk_fma_f32 v[20:21], v[20:21], v[14:15], v[14:15] op_sel:[0,0,1] op_sel_hi:[1,0,1]\n\t"
    "v_cvt_pk_bf16_f32 v194, v20, v21\n\t"
    "v_lshlrev_b32 v22, 16, v195\n\t"
    "v_and_b32 v23, s6, v195\n\t"
    "v_pk_fma_f32 v[22:23], v[22:23], v[14:15], v[14:15] op_sel:[0,0,1] op_sel_hi:[1,0,1]\n\t"
    "v_cvt_pk_bf16_f32 v195, v22, v23\n\t"
    "v_lshlrev_b32 v20, 16, v196\n\t"
    "v_and_b32 v21, s6, v196\n\t"
    "v_pk_fma_f32 v[20:21], v[20:21], v[14:15], v[14:15] op_sel:[0,0,1] op_sel_hi:[1,0,1]\n\t"
    "v_cvt_pk_bf16_f32 v196, v20, v21\n\t"
    "v_lshlrev_b32 v22, 16, v197\n\t"
    "v_and_b32 v23, s6, v197\n\t"
    "v_pk_fma_f32 v[22:23], v[22:23], v[14:15], v[14:15] op_sel:[0,0,1] op_sel_hi:[1,0,1]\n\t"
    "v_cvt_pk_bf16_f32 v197, v22, v23\n\t"
    "v_lshlrev_b32 v20, 16, v198\n\t"
    "v_and_b32 v21, s6, v198\n\t"
    "v_pk_fma_f32 v[20:21], v[20:21], v[14:15], v[14:15] op_sel:[0,0,1] op_sel_hi:[1,0,1]\n\t"
    "v_cvt_pk_bf16_f32 v198, v20, v21\n\t"
    "v_lshlrev_b32 v22, 16, v199\n\t"
    "v_and_b32 v23, s6, v199\n\t"
    "v_pk_fma_f32 v[22:23], v[22:23], v[14:15], v[14:15] op_sel:[0,0,1] op_sel_hi:[1,0,1]\n\t"
    "v_cvt_pk_bf16_f32 v199, v22, v23\n\t"
    "v_lshlrev_b32 v20, 16, v200\n\t"
    "v_and_b32 v21, s6, v200\n\t"
    "v_pk_fma_f32 v[20:21], v[20:21], v[14:15], v[14:15] op_sel:[0,0,1] op_sel_hi:[1,0,1]\n\t"
    "v_cvt_pk_bf16_f32 v200, v20, v21\n\t"
    "v_lshlrev_b32 v22, 16, v201\n\t"
    "v_and_b32 v23, s6, v201\n\t"
    "v_pk_fma_f32 v[22:23], v[22:23], v[14:15], v[14:15] op_sel:[0,0,1] op_sel_hi:[1,0,1]\n\t"
    "v_cvt_pk_bf16_f32 v201, v22, v23\n\t"
    "v_lshlrev_b32 v20, 16, v202\n\t"
    "v_and_b32 v21, s6, v202\n\t"
    "v_pk_fma_f32 v[20:21], v[20:21], v[14:15], v[14:15] op_sel:[0,0,1] op_sel_hi:[1,0,1]\n\t"
    "v_cvt_pk_bf16_f32 v202, v20, v21\n\t"
    "v_lshlrev_b32 v22, 16, v203\n\t"
    "v_and_b32 v23, s6, v203\n\t"
    "v_pk_fma_f32 v[22:23], v[22:23], v[14:15], v[14:15] op_sel:[0,0,1] op_sel_hi:[1,0,1]\n\t"
    "v_cvt_pk_bf16_f32 v203, v22, v23\n\t"
    "v_lshlrev_b32 v20, 16, v204\n\t"
    "v_and_b32 v21, s6, v204\n\t"
    "v_pk_fma_f32 v[20:21], v[20:21], v[14:15], v[14:15] op_sel:[0,0,1] op_sel_hi:[1,0,1]\n\t"
    "v_cvt_pk_bf16_f32 v204, v20, v21\n\t"
    "v_lshlrev_b32 v22, 16, v205\n\t"
    "v_and_b32 v23, s6, v205\n\t"
    "v_pk_fma_f32 v[22:23], v[22:23], v[14:15], v[14:15] op_sel:[0,0,1] op_sel_hi:[1,0,1]\n\t"
    "v_cvt_pk_bf16_f32 v205, v22, v23\n\t"
    "v_lshlrev_b32 v20, 16, v206\n\t"
    "v_and_b32 v21, s6, v206\n\t"
    "v_pk_fma_f32 v[20:21], v[20:21], v[14:15], v[14:15] op_sel:[0,0,1] op_sel_hi:[1,0,1]\n\t"
    "v_cvt_pk_bf16_f32 v206, v20, v21\n\t"
    "v_lshlrev_b32 v22, 16, v207\n\t"
    "v_and_b32 v23, s6, v207\n\t"
    "v_pk_fma_f32 v[22:23], v[22:23], v[14:15], v[14:15] op_sel:[0,0,1] op_sel_hi:[1,0,1]\n\t"
    "v_cvt_pk_bf16_f32 v207, v22, v23\n\t"
    "s_waitcnt vmcnt(8)\n\t"
    "s_barrier\n\t"
    "ds_read_b64_tr_b16 a[0:1], v1 offset:7936\n\t"
    "ds_read_b64_tr_b16 a[2:3], v1 offset:8064\n\t"
    "ds_read_b64_tr_b16 a[4:5], v1 offset:9984\n\t"
    "ds_read_b64_tr_b16 a[6:7], v1 offset:10112\n\t"
    "ds_read_b128 a[8:11], v2 offset:12032\n\t"
    "ds_read_b64_tr_b16 a[12:13], v1 offset:8000\n\t"
    "ds_read_b64_tr_b16 a[14:15], v1 offset:8128\n\t"
    "ds_read_b64_tr_b16 a[16:17], v1 offset:10048\n\t"
    "ds_read_b64_tr_b16 a[18:19], v1 offset:10176\n\t"
    "s_waitcnt lgkmcnt(5)\n\t"
    "v_mfma_f32_32x32x16_bf16 v[64:79], a[0:3], v[128:131], 0\n\t"
    "ds_read_b128 a[20:23], v2 offset:13056\n\t"
    "buffer_load_dwordx4 v5, s[24:27], s66 offen offset:2048 lds\n\t"
    "v_lshlrev_b32 v20, 16, v208\n\t"
    "v_and_b32 v21, s6, v208\n\t"
    "v_fma_f32 v20, v20, v14, v15\n\t"
    "v_mfma_f32_32x32x16_bf16 v[96:111], a[4:7], v[128:131], 0\n\t"
    "ds_read_b64_tr_b16 a[24:25], v1 offset:8448\n\t"
    "ds_read_b64_tr_b16 a[26:27], v1 offset:8576\n\t"
    "v_fma_f32 v21, v21, v14, v15\n\t"
    "v_cvt_pk_bf16_f32 v208, v20, v21\n\t"
    "s_waitcnt lgkmcnt(5)\n\t"
    "v_mfma_f32_32x32x16_bf16 v[32:47], v[128:131], a[8:11], 0\n\t"
    "ds_read_b64_tr_b16 a[28:29], v1 offset:10496\n\t"
    "ds_read_b64_tr_b16 a[30:31], v1 offset:10624\n\t"
    "buffer_load_dwordx4 v6, s[24:27], s66 offen offset:3072 lds\n\t"
    "v_lshlrev_b32 v22, 16, v209\n\t"
    "v_and_b32 v23, s6, v209\n\t"
    "v_mfma_f32_32x32x16_bf16 v[80:95], a[12:15], v[128:131], 0\n\t"
    "ds_read_b128 a[0:3], v3 offset:12032\n\t"
    "v_fma_f32 v22, v22, v14, v15\n\t"
    "v_fma_f32 v23, v23, v14, v15\n\t"
    "v_cvt_pk_bf16_f32 v209, v22, v23\n\t"
    "s_waitcnt lgkmcnt(5)\n\t"
    "v_mfma_f32_32x32x16_bf16 v[112:127], a[16:19], v[128:131], 0\n\t"
    "s_add_u32 m0, s52, 16384\n\t"
    "ds_read_b64_tr_b16 a[4:5], v1 offset:8512\n\t"
    "ds_read_b64_tr_b16 a[6:7], v1 offset:8640\n\t"
    "buffer_load_dwordx4 v4, s[12:15], s58 offen lds\n\t"
    "v_lshlrev_b32 v20, 16, v210\n\t"
    "v_and_b32 v21, s6, v210\n\t"
    "v_mfma_f32_32x32x16_bf16 v[48:63], v[128:131], a[20:23], 0\n\t"
    "ds_read_b64_tr_b16 a[8:9], v1 offset:10560\n\t"
    "ds_read_b64_tr_b16 a[10:11], v1 offset:10688\n\t"
    "v_fma_f32 v20, v20, v14, v15\n\t"
    "v_fma_f32 v21, v21, v14, v15\n\t"
    "s_waitcnt lgkmcnt(5)\n\t"
    "v_mfma_f32_32x32x16_bf16 v[64:79], a[24:27], v[132:135], v[64:79]\n\t"
    "ds_read_b128 a[12:15], v3 offset:13056\n\t"
    "buffer_load_dwordx4 v4, s[16:19], s58 offen offset:1024 lds\n\t"
    "v_cvt_pk_bf16_f32 v210, v20, v21\n\t"
    "v_lshlrev_b32 v22, 16, v211\n\t"
    "v_and_b32 v23, s6, v211\n\t"
    "v_mfma_f32_32x32x16_bf16 v[96:111], a[28:31], v[132:135], v[96:111]\n\t"
    "ds_read_b64_tr_b16 a[16:17], v1 offset:8960\n\t"
    "ds_read_b64_tr_b16 a[18:19], v1 offset:9088\n\t"
    "v_fma_f32 v22, v22, v14, v15\n\t"
    "v_fma_f32 v23, v23, v14, v15\n\t"
    "s_waitcnt lgkmcnt(5)\n\t"
    "v_mfma_f32_32x32x16_bf16 v[32:47], v[132:135], a[0:3], v[32:47]\n\t"
    "s_add_u32 m0, s52, 18432\n\t"
    "ds_read_b64_tr_b16 a[20:21], v1 offset:11008\n\t"
    "ds_read_b64_tr_b16 a[22:23], v1 offset:11136\n\t"
    "buffer_load_dwordx4 v5, s[20:23], s67 offen lds\n\t"
    "v_cvt_pk_bf16_f32 v211, v22, v23\n\t"
    "v_lshlrev_b32 v20, 16, v212\n\t"
    "v_mfma_f32_32x32x16_bf16 v[80:95], a[4:7], v[132:135], v[80:95]\n\t"
    "ds_read_b128 a[24:27], v2 offset:12096\n\t"
    "v_and_b32 v21, s6, v212\n\t"
    "v_fma_f32 v20, v20, v14, v15\n\t"
    "v_fma_f32 v21, v21, v14, v15\n\t"
    "s_waitcnt lgkmcnt(5)\n\t"
    "v_mfma_f32_32x32x16_bf16 v[112:127], a[8:11], v[132:135], v[112:127]\n\t"
    "ds_read_b64_tr_b16 a[28:29], v1 offset:9024\n\t"
    "ds_read_b64_tr_b16 a[30:31], v1 offset:9152\n\t"
    "buffer_load_dwordx4 v6, s[20:23], s67 offen offset:1024 lds\n\t"
    "v_cvt_pk_bf16_f32 v212, v20, v21\n\t"
    "v_lshlrev_b32 v22, 16, v213\n\t"
    "v_mfma_f32_32x32x16_bf16 v[48:63], v[132:135], a[12:15], v[48:63]\n\t"
    "ds_read_b64_tr_b16 a[0:1], v1 offset:11072\n\t"
    "ds_read_b64_tr_b16 a[2:3], v1 offset:11200\n\t"
    "v_and_b32 v23, s6, v213\n\t"
    "v_fma_f32 v22, v22, v14, v15\n\t"
    "s_waitcnt lgkmcnt(5)\n\t"
    "v_mfma_f32_32x32x16_bf16 v[64:79], a[16:19], v[136:139], v[64:79]\n\t"
    "ds_read_b128 a[4:7], v2 offset:13120\n\t"
    "buffer_load_dwordx4 v5, s[24:27], s67 offen offset:2048 lds\n\t"
    "v_fma_f32 v23, v23, v14, v15\n\t"
    "v_cvt_pk_bf16_f32 v213, v22, v23\n\t"
    "v_lshlrev_b32 v20, 16, v214\n\t"
    "v_mfma_f32_32x32x16_bf16 v[96:111], a[20:23], v[136:139], v[96:111]\n\t"
    "ds_read_b64_tr_b16 a[8:9], v1 offset:9472\n\t"
    "ds_read_b64_tr_b16 a[10:11], v1 offset:9600\n\t"
    "v_and_b32 v21, s6, v214\n\t"
    "v_fma_f32 v20, v20, v14, v15\n\t"
    "s_waitcnt lgkmcnt(5)\n\t"
    "v_mfma_f32_32x32x16_bf16 v[32:47], v[136:139], a[24:27], v[32:47]\n\t"
    "ds_read_b64_tr_b16 a[12:13], v1 offset:11520\n\t"
    "ds_read_b64_tr_b16 a[14:15], v1 offset:11648\n\t"
    "buffer_load_dwordx4 v6, s[24:27], s67 offen offset:3072 lds\n\t"
    "v_fma_f32 v21, v21, v14, v15\n\t"
    "v_cvt_pk_bf16_f32 v214, v20, v21\n\t"
    "v_mfma_f32_32x32x16_bf16 v[80:95], a[28:31], v[136:139], v[80:95]\n\t"
    "ds_read_b128 a[16:19], v3 offset:12096\n\t"
    "v_lshlrev_b32 v22, 16, v215\n\t"
    "v_and_b32 v23, s6, v215\n\t"
    "v_fma_f32 v22, v22, v14, v15\n\t"
    "s_waitcnt lgkmcnt(5)\n\t"
    "v_mfma_f32_32x32x16_bf16 v[112:127], a[0:3], v[136:139], v[112:127]\n\t"
    "s_add_u32 m0, s52, 22528\n\t"
    "ds_read_b64_tr_b16 a[20:21], v1 offset:9536\n\t"
    "ds_read_b64_tr_b16 a[22:23], v1 offset:9664\n\t"
    "buffer_load_dwordx4 v4, s[12:15], s59 offen lds\n\t"
    "v_fma_f32 v23, v23, v14, v15\n\t"
    "v_cvt_pk_bf16_f32 v215, v22, v23\n\t"
    "v_mfma_f32_32x32x16_bf16 v[48:63], v[136:139], a[4:7], v[48:63]\n\t"
    "ds_read_b64_tr_b16 a[24:25], v1 offset:11584\n\t"
    "ds_read_b64_tr_b16 a[26:27], v1 offset:11712\n\t"
    "v_lshlrev_b32 v20, 16, v216\n\t"
    "v_and_b32 v21, s6, v216\n\t"
    "s_waitcnt lgkmcnt(5)\n\t"
    "v_mfma_f32_32x32x16_bf16 v[64:79], a[8:11], v[140:143], v[64:79]\n\t"
    "ds_read_b128 a[28:31], v3 offset:13120\n\t"
    "buffer_load_dwordx4 v4, s[16:19], s59 offen offset:1024 lds\n\t"
    "v_fma_f32 v20, v20, v14, v15\n\t"
    "v_fma_f32 v21, v21, v14, v15\n\t"
    "v_cvt_pk_bf16_f32 v216, v20, v21\n\t"
    "v_mfma_f32_32x32x16_bf16 v[96:111], a[12:15], v[140:143], v[96:111]\n\t"
    "s_waitcnt vmcnt(12)\n\t"
    "s_barrier\n\t"
    "ds_read_b64_tr_b16 a[0:1], v1 offset:14080\n\t"
    "ds_read_b64_tr_b16 a[2:3], v1 offset:14208\n\t"
    "v_lshlrev_b32 v22, 16, v217\n\t"
    "v_and_b32 v23, s6, v217\n\t"
    "s_waitcnt lgkmcnt(5)\n\t"
    "v_mfma_f32_32x32x16_bf16 v[32:47], v[140:143], a[16:19], v[32:47]\n\t"
    "s_add_u32 m0, s52, 24576\n\t"
    "ds_read_b64_tr_b16 a[4:5], v1 offset:16128\n\t"
    "ds_read_b64_tr_b16 a[6:7], v1 offset:16256\n\t"
    "buffer_load_dwordx4 v4, s[12:15], s60 offen lds\n\t"
    "v_fma_f32 v22, v22, v14, v15\n\t"
    "v_fma_f32 v23, v23, v14, v15\n\t"
    "v_mfma_f32_32x32x16_bf16 v[80:95], a[20:23], v[140:143], v[80:95]\n\t"
    "ds_read_b128 a[8:11], v2 offset:18176\n\t"
    "v_cvt_pk_bf16_f32 v217, v22, v23\n\t"
    "v_lshlrev_b32 v20, 16, v218\n\t"
    "v_and_b32 v21, s6, v218\n\t"
    "s_waitcnt lgkmcnt(5)\n\t"
    "v_mfma_f32_32x32x16_bf16 v[112:127], a[24:27], v[140:143], v[112:127]\n\t"
    "ds_read_b64_tr_b16 a[12:13], v1 offset:14144\n\t"
    "ds_read_b64_tr_b16 a[14:15], v1 offset:14272\n\t"
    "buffer_load_dwordx4 v4, s[16:19], s60 offen offset:1024 lds\n\t"
    "v_fma_f32 v20, v20, v14, v15\n\t"
    "v_fma_f32 v21, v21, v14, v15\n\t"
    "v_mfma_f32_32x32x16_bf16 v[48:63], v[140:143], a[28:31], v[48:63]\n\t"
    "ds_read_b64_tr_b16 a[16:17], v1 offset:16192\n\t"
    "ds_read_b64_tr_b16 a[18:19], v1 offset:16320\n\t"
    "v_cvt_pk_bf16_f32 v218, v20, v21\n\t"
    "v_lshlrev_b32 v22, 16, v219\n\t"
    "s_waitcnt lgkmcnt(5)\n\t"
    "v_mfma_f32_32x32x16_bf16 v[64:79], a[0:3], v[144:147], v[64:79]\n\t"
    "s_add_u32 m0, s52, 32768\n\t"
    "ds_read_b128 a[20:23], v2 offset:19200\n\t"
    "buffer_load_dwordx4 v4, s[12:15], s61 offen lds\n\t"
    "v_and_b32 v23, s6, v219\n\t"
    "v_fma_f32 v22, v22, v14, v15\n\t"
    "v_mfma_f32_32x32x16_bf16 v[96:111], a[4:7], v[144:147], v[96:111]\n\t"
    "ds_read_b64_tr_b16 a[24:25], v1 offset:14592\n\t"
    "ds_read_b64_tr_b16 a[26:27], v1 offset:14720\n\t"
    "v_fma_f32 v23, v23, v14, v15\n\t"
    "v_cvt_pk_bf16_f32 v219, v22, v23\n\t"
    "s_waitcnt lgkmcnt(5)\n\t"
    "v_mfma_f32_32x32x16_bf16 v[32:47], v[144:147], a[8:11], v[32:47]\n\t"
    "ds_read_b64_tr_b16 a[28:29], v1 offset:16640\n\t"
    "ds_read_b64_tr_b16 a[30:31], v1 offset:16768\n\t"
    "buffer_load_dwordx4 v4, s[16:19], s61 offen offset:1024 lds\n\t"
    "v_lshlrev_b32 v20, 16, v220\n\t"
    "v_and_b32 v21, s6, v220\n\t"
    "v_mfma_f32_32x32x16_bf16 v[80:95], a[12:15], v[144:147], v[80:95]\n\t"
    "ds_read_b128 a[0:3], v3 offset:18176\n\t"
    "v_fma_f32 v20, v20, v14, v15\n\t"
    "v_fma_f32 v21, v21, v14, v15\n\t"
    "v_cvt_pk_bf16_f32 v220, v20, v21\n\t"
    "s_waitcnt lgkmcnt(5)\n\t"
    "v_mfma_f32_32x32x16_bf16 v[112:127], a[16:19], v[144:147], v[112:127]\n\t"
    "s_add_u32 m0, s52, 34816\n\t"
    "ds_read_b64_tr_b16 a[4:5], v1 offset:14656\n\t"
    "ds_read_b64_tr_b16 a[6:7], v1 offset:14784\n\t"
    "buffer_load_dwordx4 v4, s[12:15], s62 offen lds\n\t"
    "v_lshlrev_b32 v22, 16, v221\n\t"
    "v_and_b32 v23, s6, v221\n\t"
    "v_mfma_f32_32x32x16_bf16 v[48:63], v[144:147], a[20:23], v[48:63]\n\t"
    "ds_read_b64_tr_b16 a[8:9], v1 offset:16704\n\t"
    "ds_read_b64_tr_b16 a[10:11], v1 offset:16832\n\t"
    "v_fma_f32 v22, v22, v14, v15\n\t"
    "v_fma_f32 v23, v23, v14, v15\n\t"
    "s_waitcnt lgkmcnt(5)\n\t"
    "v_mfma_f32_32x32x16_bf16 v[64:79], a[24:27], v[148:151], v[64:79]\n\t"
    "ds_read_b128 a[12:15], v3 offset:19200\n\t"
    "buffer_load_dwordx4 v4, s[16:19], s62 offen offset:1024 lds\n\t"
    "v_cvt_pk_bf16_f32 v221, v22, v23\n\t"
    "v_lshlrev_b32 v20, 16, v222\n\t"
    "v_and_b32 v21, s6, v222\n\t"
    "v_mfma_f32_32x32x16_bf16 v[96:111], a[28:31], v[148:151], v[96:111]\n\t"
    "ds_read_b64_tr_b16 a[16:17], v1 offset:15104\n\t"
    "ds_read_b64_tr_b16 a[18:19], v1 offset:15232\n\t"
    "v_fma_f32 v20, v20, v14, v15\n\t"
    "v_fma_f32 v21, v21, v14, v15\n\t"
    "s_waitcnt lgkmcnt(5)\n\t"
    "v_mfma_f32_32x32x16_bf16 v[32:47], v[148:151], a[0:3], v[32:47]\n\t"
    "s_add_u32 m0, s52, 26624\n\t"
    "ds_read_b64_tr_b16 a[20:21], v1 offset:17152\n\t"
    "ds_read_b64_tr_b16 a[22:23], v1 offset:17280\n\t"
    "buffer_load_dwordx4 v4, s[12:15], s63 offen lds\n\t"
    "v_cvt_pk_bf16_f32 v222, v20, v21\n\t"
    "v_lshlrev_b32 v22, 16, v223\n\t"
    "v_mfma_f32_32x32x16_bf16 v[80:95], a[4:7], v[148:151], v[80:95]\n\t"
    "ds_read_b128 a[24:27], v2 offset:18240\n\t"
    "v_and_b32 v23, s6, v223\n\t"
    "v_fma_f32 v22, v22, v14, v15\n\t"
    "v_fma_f32 v23, v23, v14, v15\n\t"
    "s_waitcnt lgkmcnt(5)\n\t"
    "v_mfma_f32_32x32x16_bf16 v[112:127], a[8:11], v[148:151], v[112:127]\n\t"
    "ds_read_b64_tr_b16 a[28:29], v1 offset:15168\n\t"
    "ds_read_b64_tr_b16 a[30:31], v1 offset:15296\n\t"
    "buffer_load_dwordx4 v4, s[16:19], s63 offen offset:1024 lds\n\t"
    "v_cvt_pk_bf16_f32 v223, v22, v23\n\t"
    "v_lshlrev_b32 v20, 16, v224\n\t"
    "v_mfma_f32_32x32x16_bf16 v[48:63], v[148:151], a[12:15], v[48:63]\n\t"
    "ds_read_b64_tr_b16 a[0:1], v1 offset:17216\n\t"
    "Lpad1_%=:\n\t"
    "s_cbranch_execz Lpad5_%=\n\t"
    "ds_read_b64_tr_b16 a[2:3], v1 offset:17344\n\t"
    "v_and_b32 v21, s6, v224\n\t"
    "v_fma_f32 v20, v20, v14, v15\n\t"
    "s_waitcnt lgkmcnt(5)\n\t"
    "v_mfma_f32_32x32x16_bf16 v[64:79], a[16:19], v[152:155], v[64:79]\n\t"
    "s_add_u32 m0, s52, 36864\n\t"
    "ds_read_b128 a[4:7], v2 offset:19264\n\t"
    "buffer_load_dwordx4 v5, s[20:23], s68 offen lds\n\t"
    "v_fma_f32 v21, v21, v14, v15\n\t"
    "v_cvt_pk_bf16_f32 v224, v20, v21\n\t"
    "v_mfma_f32_32x32x16_bf16 v[96:111], a[20:23], v[152:155], v[96:111]\n\t"
    "ds_read_b64_tr_b16 a[8:9], v1 offset:15616\n\t"
    "ds_read_b64_tr_b16 a[10:11], v1 offset:15744\n\t"
    "v_lshlrev_b32 v22, 16, v225\n\t"
    "v_and_b32 v23, s6, v225\n\t"
    "s_waitcnt lgkmcnt(5)\n\t"
    "v_mfma_f32_32x32x16_bf16 v[32:47], v[152:155], a[24:27], v[32:47]\n\t"
    "ds_read_b64_tr_b16 a[12:13], v1 offset:17664\n\t"
    "ds_read_b64_tr_b16 a[14:15], v1 offset:17792\n\t"
    "buffer_load_dwordx4 v6, s[20:23], s68 offen offset:1024 lds\n\t"
    "v_fma_f32 v22, v22, v14, v15\n\t"
    "v_fma_f32 v23, v23, v14, v15\n\t"
    "v_mfma_f32_32x32x16_bf16 v[80:95], a[28:31], v[152:155], v[80:95]\n\t"
    "ds_read_b128 a[16:19], v3 offset:18240\n\t"
    "v_cvt_pk_bf16_f32 v225, v22, v23\n\t"
    "v_lshlrev_b32 v20, 16, v226\n\t"
    "v_and_b32 v21, s6, v226\n\t"
    "s_waitcnt lgkmcnt(5)\n\t"
    "v_mfma_f32_32x32x16_bf16 v[112:127], a[0:3], v[152:155], v[112:127]\n\t"
    "s_sub_u32 m0, s52, 2048\n\t"
    "ds_read_b64_tr_b16 a[20:21], v1 offset:15680\n\t"
    "ds_read_b64_tr_b16 a[22:23], v1 offset:15808\n\t"
    "buffer_load_dwordx4 v5, s[24:27], s68 offen offset:2048 lds\n\t"
    "v_fma_f32 v20, v20, v14, v15\n\t"
    "v_fma_f32 v21, v21, v14, v15\n\t"
    "v_mfma_f32_32x32x16_bf16 v[48:63], v[152:155], a[4:7], v[48:63]\n\t"
    "ds_read_b64_tr_b16 a[24:25], v1 offset:17728\n\t"
    "ds_read_b64_tr_b16 a[26:27], v1 offset:17856\n\t"
    "v_cvt_pk_bf16_f32 v226, v20, v21\n\t"
    "v_lshlrev_b32 v22, 16, v227\n\t"
    "s_waitcnt lgkmcnt(5)\n\t"
    "v_mfma_f32_32x32x16_bf16 v[64:79], a[8:11], v[156:159], v[64:79]\n\t"
    "s_sub_u32 m0, s52, 1024\n\t"
    "ds_read_b128 a[28:31], v3 offset:19264\n\t"
    "buffer_load_dwordx4 v6, s[24:27], s68 offen offset:3072 lds\n\t"
    "v_and_b32 v23, s6, v227\n\t"
    "v_fma_f32 v22, v22, v14, v15\n\t"
    "v_mfma_f32_32x32x16_bf16 v[96:111], a[12:15], v[156:159], v[96:111]\n\t"
    "s_waitcnt vmcnt(15)\n\t"
    "s_barrier\n\t"
    "ds_read_b64_tr_b16 a[0:1], v1 offset:20224\n\t"
    "ds_read_b64_tr_b16 a[2:3], v1 offset:20352\n\t"
    "v_fma_f32 v23, v23, v14, v15\n\t"
    "v_cvt_pk_bf16_f32 v227, v22, v23\n\t"
    "s_waitcnt lgkmcnt(5)\n\t"
    "v_mfma_f32_32x32x16_bf16 v[32:47], v[156:159], a[16:19], v[32:47]\n\t"
    "s_add_u32 m0, s52, 3072\n\t"
    "ds_read_b64_tr_b16 a[4:5], v1 offset:22272\n\t"
    "ds_read_b64_tr_b16 a[6:7], v1 offset:22400\n\t"
    "buffer_load_dwordx4 v5, s[20:23], s69 offen lds\n\t"
    "v_lshlrev_b32 v20, 16, v228\n\t"
    "v_and_b32 v21, s6, v228\n\t"
    "v_mfma_f32_32x32x16_bf16 v[80:95], a[20:23], v[156:159], v[80:95]\n\t"
    "ds_read_b128 a[8:11], v2 offset:24320\n\t"
    "v_fma_f32 v20, v20, v14, v15\n\t"
    "v_fma_f32 v21, v21, v14, v15\n\t"
    "v_cvt_pk_bf16_f32 v228, v20, v21\n\t"
    "s_waitcnt lgkmcnt(5)\n\t"
    "v_mfma_f32_32x32x16_bf16 v[112:127], a[24:27], v[156:159], v[112:127]\n\t"
    "ds_read_b64_tr_b16 a[12:13], v1 offset:20288\n\t"
    "ds_read_b64_tr_b16 a[14:15], v1 offset:20416\n\t"
    "buffer_load_dwordx4 v6, s[20:23], s69 offen offset:1024 lds\n\t"
    "v_lshlrev_b32 v22, 16, v229\n\t"
    "v_and_b32 v23, s6, v229\n\t"
    "v_mfma_f32_32x32x16_bf16 v[48:63], v[156:159], a[28:31], v[48:63]\n\t"
    "ds_read_b64_tr_b16 a[16:17], v1 offset:22336\n\t"
    "ds_read_b64_tr_b16 a[18:19], v1 offset:22464\n\t"
    "v_fma_f32 v22, v22, v14, v15\n\t"
    "v_fma_f32 v23, v23, v14, v15\n\t"
    "s_waitcnt lgkmcnt(5)\n\t"
    "v_mfma_f32_32x32x16_bf16 v[64:79], a[0:3], v[160:163], v[64:79]\n\t"
    "ds_read_b128 a[20:23], v2 offset:25344\n\t"
    "buffer_load_dwordx4 v5, s[24:27], s69 offen offset:2048 lds\n\t"
    "v_cvt_pk_bf16_f32 v229, v22, v23\n\t"
    "v_lshlrev_b32 v20, 16, v230\n\t"
    "v_and_b32 v21, s6, v230\n\t"
    "v_mfma_f32_32x32x16_bf16 v[96:111], a[4:7], v[160:163], v[96:111]\n\t"
    "ds_read_b64_tr_b16 a[24:25], v1 offset:20736\n\t"
    "ds_read_b64_tr_b16 a[26:27], v1 offset:20864\n\t"
    "v_fma_f32 v20, v20, v14, v15\n\t"
    "v_fma_f32 v21, v21, v14, v15\n\t"
    "s_waitcnt lgkmcnt(5)\n\t"
    "v_mfma_f32_32x32x16_bf16 v[32:47], v[160:163], a[8:11], v[32:47]\n\t"
    "ds_read_b64_tr_b16 a[28:29], v1 offset:22784\n\t"
    "ds_read_b64_tr_b16 a[30:31], v1 offset:22912\n\t"
    "buffer_load_dwordx4 v6, s[24:27], s69 offen offset:3072 lds\n\t"
    "v_cvt_pk_bf16_f32 v230, v20, v21\n\t"
    "v_lshlrev_b32 v22, 16, v231\n\t"
    "v_mfma_f32_32x32x16_bf16 v[80:95], a[12:15], v[160:163], v[80:95]\n\t"
    "ds_read_b128 a[0:3], v3 offset:24320\n\t"
    "v_and_b32 v23, s6, v231\n\t"
    "v_fma_f32 v22, v22, v14, v15\n\t"
    "v_fma_f32 v23, v23, v14, v15\n\t"
    "s_waitcnt lgkmcnt(5)\n\t"
    "v_mfma_f32_32x32x16_bf16 v[112:127], a[16:19], v[160:163], v[112:127]\n\t"
    "s_add_u32 m0, s52, 1024\n\t"
    "ds_read_b64_tr_b16 a[4:5], v1 offset:20800\n\t"
    "ds_read_b64_tr_b16 a[6:7], v1 offset:20928\n\t"
    "buffer_load_dwordx4 v5, s[20:23], s70 offen lds\n\t"
    "v_cvt_pk_bf16_f32 v231, v22, v23\n\t"
    "v_lshlrev_b32 v20, 16, v232\n\t"
    "v_mfma_f32_32x32x16_bf16 v[48:63], v[160:163], a[20:23], v[48:63]\n\t"
    "ds_read_b64_tr_b16 a[8:9], v1 offset:22848\n\t"
    "ds_read_b64_tr_b16 a[10:11], v1 offset:22976\n\t"
    "v_and_b32 v21, s6, v232\n\t"
    "v_fma_f32 v20, v20, v14, v15\n\t"
    "s_waitcnt lgkmcnt(5)\n\t"
    "v_mfma_f32_32x32x16_bf16 v[64:79], a[24:27], v[164:167], v[64:79]\n\t"
    "s_add_u32 m0, s52, 7168\n\t"
    "ds_read_b128 a[12:15], v3 offset:25344\n\t"
    "buffer_load_dwordx4 v6, s[20:23], s70 offen offset:1024 lds\n\t"
    "v_fma_f32 v21, v21, v14, v15\n\t"
    "v_cvt_pk_bf16_f32 v232, v20, v21\n\t"
    "v_mfma_f32_32x32x16_bf16 v[96:111], a[28:31], v[164:167], v[96:111]\n\t"
    "ds_read_b64_tr_b16 a[16:17], v1 offset:21248\n\t"
    "ds_read_b64_tr_b16 a[18:19], v1 offset:21376\n\t"
    "v_lshlrev_b32 v22, 16, v233\n\t"
    "v_and_b32 v23, s6, v233\n\t"
    "s_waitcnt lgkmcnt(5)\n\t"
    "v_mfma_f32_32x32x16_bf16 v[32:47], v[164:167], a[0:3], v[32:47]\n\t"
    "s_add_u32 m0, s52, 26624\n\t"
    "ds_read_b64_tr_b16 a[20:21], v1 offset:23296\n\t"
    "ds_read_b64_tr_b16 a[22:23], v1 offset:23424\n\t"
    "buffer_load_dwordx4 v5, s[24:27], s70 offen offset:2048 lds\n\t"
    "v_fma_f32 v22, v22, v14, v15\n\t"
    "v_fma_f32 v23, v23, v14, v15\n\t"
    "v_mfma_f32_32x32x16_bf16 v[80:95], a[4:7], v[164:167], v[80:95]\n\t"
    "s_waitcnt vmcnt(15)\n\t"
    "s_barrier\n\t"
    "ds_read_b128 a[24:27], v2 offset:24384\n\t"
    "v_cvt_pk_bf16_f32 v233, v22, v23\n\t"
    "v_lshlrev_b32 v20, 16, v234\n\t"
    "s_waitcnt lgkmcnt(5)\n\t"
    "v_mfma_f32_32x32x16_bf16 v[112:127], a[8:11], v[164:167], v[112:127]\n\t"
    "s_add_u32 m0, s52, 4096\n\t"
    "ds_read_b64_tr_b16 a[28:29], v1 offset:21312\n\t"
    "ds_read_b64_tr_b16 a[30:31], v1 offset:21440\n\t"
    "buffer_load_dwordx4 v6, s[24:27], s70 offen offset:3072 lds\n\t"
    "v_and_b32 v21, s6, v234\n\t"
    "v_fma_f32 v20, v20, v14, v15\n\t"
    "v_mfma_f32_32x32x16_bf16 v[48:63], v[164:167], a[12:15], v[48:63]\n\t"
    "ds_read_b64_tr_b16 a[0:1], v1 offset:23360\n\t"
    "ds_read_b64_tr_b16 a[2:3], v1 offset:23488\n\t"
    "v_fma_f32 v21, v21, v14, v15\n\t"
    "v_cvt_pk_bf16_f32 v234, v20, v21\n\t"
    "s_waitcnt lgkmcnt(5)\n\t"
    "v_mfma_f32_32x32x16_bf16 v[64:79], a[16:19], v[168:171], v[64:79]\n\t"
    "s_add_u32 m0, s52, 9216\n\t"
    "ds_read_b128 a[4:7], v2 offset:25408\n\t"
    "buffer_load_dwordx4 v5, s[20:23], s71 offen lds\n\t"
    "v_lshlrev_b32 v22, 16, v235\n\t"
    "v_and_b32 v23, s6, v235\n\t"
    "v_mfma_f32_32x32x16_bf16 v[96:111], a[20:23], v[168:171], v[96:111]\n\t"
    "ds_read_b64_tr_b16 a[8:9], v1 offset:21760\n\t"
    "ds_read_b64_tr_b16 a[10:11], v1 offset:21888\n\t"
    "v_fma_f32 v22, v22, v14, v15\n\t"
    "v_fma_f32 v23, v23, v14, v15\n\t"
    "s_waitcnt lgkmcnt(5)\n\t"
    "v_mfma_f32_32x32x16_bf16 v[32:47], v[168:171], a[24:27], v[32:47]\n\t"
    "ds_read_b64_tr_b16 a[12:13], v1 offset:23808\n\t"
    "ds_read_b64_tr_b16 a[14:15], v1 offset:23936\n\t"
    "buffer_load_dwordx4 v6, s[20:23], s71 offen offset:1024 lds\n\t"
    "v_cvt_pk_bf16_f32 v235, v22, v23\n\t"
    "v_lshlrev_b32 v20, 16, v236\n\t"
    "v_mfma_f32_32x32x16_bf16 v[80:95], a[28:31], v[168:171], v[80:95]\n\t"
    "ds_read_b128 a[16:19], v3 offset:24384\n\t"
    "v_and_b32 v21, s6, v236\n\t"
    "v_fma_f32 v20, v20, v14, v15\n\t"
    "v_fma_f32 v21, v21, v14, v15\n\t"
    "s_waitcnt lgkmcnt(5)\n\t"
    "v_mfma_f32_32x32x16_bf16 v[112:127], a[0:3], v[168:171], v[112:127]\n\t"
    "ds_read_b64_tr_b16 a[20:21], v1 offset:21824\n\t"
    "ds_read_b64_tr_b16 a[22:23], v1 offset:21952\n\t"
    "buffer_load_dwordx4 v5, s[24:27], s71 offen offset:2048 lds\n\t"
    "v_cvt_pk_bf16_f32 v236, v20, v21\n\t"
    "v_lshlrev_b32 v22, 16, v237\n\t"
    "v_mfma_f32_32x32x16_bf16 v[48:63], v[168:171], a[4:7], v[48:63]\n\t"
    "ds_read_b64_tr_b16 a[24:25], v1 offset:23872\n\t"
    "ds_read_b64_tr_b16 a[26:27], v1 offset:24000\n\t"
    "v_and_b32 v23, s6, v237\n\t"
    "v_fma_f32 v22, v22, v14, v15\n\t"
    "s_waitcnt lgkmcnt(5)\n\t"
    "v_mfma_f32_32x32x16_bf16 v[64:79], a[8:11], v[172:175], v[64:79]\n\t"
    "ds_read_b128 a[28:31], v3 offset:25408\n\t"
    "buffer_load_dwordx4 v6, s[24:27], s71 offen offset:3072 lds\n\t"
    "v_fma_f32 v23, v23, v14, v15\n\t"
    "v_cvt_pk_bf16_f32 v237, v22, v23\n\t"
    "v_lshlrev_b32 v20, 16, v238\n\t"
    "v_mfma_f32_32x32x16_bf16 v[96:111], a[12:15], v[172:175], v[96:111]\n\t"
    "ds_read_b64_tr_b16 a[0:1], v1 offset:26368\n\t"
    "ds_read_b64_tr_b16 a[2:3], v1 offset:26496\n\t"
    "v_and_b32 v21, s6, v238\n\t"
    "v_fma_f32 v20, v20, v14, v15\n\t"
    "s_waitcnt lgkmcnt(5)\n\t"
    "v_mfma_f32_32x32x16_bf16 v[32:47], v[172:175], a[16:19], v[32:47]\n\t"
    "ds_read_b64_tr_b16 a[4:5], v1 offset:28416\n\t"
    "ds_read_b64_tr_b16 a[6:7], v1 offset:28544\n\t"
    "v_fma_f32 v21, v21, v14, v15\n\t"
    "v_cvt_pk_bf16_f32 v238, v20, v21\n\t"
    "v_lshlrev_b32 v22, 16, v239\n\t"
    "v_mfma_f32_32x32x16_bf16 v[80:95], a[20:23], v[172:175], v[80:95]\n\t"
    "ds_read_b128 a[8:11], v2 offset:30464\n\t"
    "v_and_b32 v23, s6, v239\n\t"
    "v_fma_f32 v22, v22, v14, v15\n\t"
    "v_fma_f32 v23, v23, v14, v15\n\t"
    "s_waitcnt lgkmcnt(5)\n\t"
    "v_mfma_f32_32x32x16_bf16 v[112:127], a[24:27], v[172:175], v[112:127]\n\t"
    "s_waitcnt vmcnt(13)\n\t"
    "s_barrier\n\t"
    "s_add_u32 m0, s52, 13312\n\t"
    "ds_read_b64_tr_b16 a[12:13], v1 offset:26432\n\t"
    "ds_read_b64_tr_b16 a[14:15], v1 offset:26560\n\t"
    "buffer_load_dwordx4 v8, s[20:23], s64 offen lds\n\t"
    "v_cvt_pk_bf16_f32 v239, v22, v23\n\t"
    "v_lshlrev_b32 v20, 16, v240\n\t"
    "v_mfma_f32_32x32x16_bf16 v[48:63], v[172:175], a[28:31], v[48:63]\n\t"
    "ds_read_b64_tr_b16 a[16:17], v1 offset:28480\n\t"
    "ds_read_b64_tr_b16 a[18:19], v1 offset:28608\n\t"
    "v_and_b32 v21, s6, v240\n\t"
    "v_fma_f32 v20, v20, v14, v15\n\t"
    "s_waitcnt lgkmcnt(5)\n\t"
    "v_mfma_f32_32x32x16_bf16 v[64:79], a[0:3], v[176:179], v[64:79]\n\t"
    "ds_read_b128 a[20:23], v2 offset:31488\n\t"
    "buffer_load_dwordx4 v9, s[20:23], s64 offen offset:1024 lds\n\t"
    "v_fma_f32 v21, v21, v14, v15\n\t"
    "v_cvt_pk_bf16_f32 v240, v20, v21\n\t"
    "v_lshlrev_b32 v22, 16, v241\n\t"
    "v_mfma_f32_32x32x16_bf16 v[96:111], a[4:7], v[176:179], v[96:111]\n\t"
    "ds_read_b64_tr_b16 a[24:25], v1 offset:26880\n\t"
    "ds_read_b64_tr_b16 a[26:27], v1 offset:27008\n\t"
    "v_and_b32 v23, s6, v241\n\t"
    "v_fma_f32 v22, v22, v14, v15\n\t"
    "s_waitcnt lgkmcnt(5)\n\t"
    "v_mfma_f32_32x32x16_bf16 v[32:47], v[176:179], a[8:11], v[32:47]\n\t"
    "ds_read_b64_tr_b16 a[28:29], v1 offset:28928\n\t"
    "ds_read_b64_tr_b16 a[30:31], v1 offset:29056\n\t"
    "buffer_load_dwordx4 v8, s[24:27], s64 offen offset:2048 lds\n\t"
    "v_fma_f32 v23, v23, v14, v15\n\t"
    "v_cvt_pk_bf16_f32 v241, v22, v23\n\t"
    "v_mfma_f32_32x32x16_bf16 v[80:95], a[12:15], v[176:179], v[80:95]\n\t"
    "ds_read_b128 a[0:3], v3 offset:30464\n\t"
    "v_lshlrev_b32 v20, 16, v242\n\t"
    "v_and_b32 v21, s6, v242\n\t"
    "v_fma_f32 v20, v20, v14, v15\n\t"
    "s_waitcnt lgkmcnt(5)\n\t"
    "v_mfma_f32_32x32x16_bf16 v[112:127], a[16:19], v[176:179], v[112:127]\n\t"
    "ds_read_b64_tr_b16 a[4:5], v1 offset:26944\n\t"
    "ds_read_b64_tr_b16 a[6:7], v1 offset:27072\n\t"
    "buffer_load_dwordx4 v9, s[24:27], s64 offen offset:3072 lds\n\t"
    "s_cmp_lg_u32 s94, 3\n\t"
    "s_cbranch_scc1 Lo35_%=\n\t"
    "s_and_b32 s97, s92, 7\n\t"
    "s_lshl2_add_u32 s97, s97, s95\n\t"
    "s_lshl_b32 s4, s93, 5\n\t"
    "s_add_u32 s4, s4, s97\n\t"
    "s_mul_i32 s4, s4, 14336\n\t"
    "s_add_u32 s4, s4, 0\n\t"
    "v_min_u32 v17, 432, v0\n\t"
    "v_lshlrev_b32 v17, 3, v17\n\t"
    "buffer_load_dword a53, v17, s[32:35], s4 offen\n\t"
    "Lo35_%=:\n\t"
    "v_fma_f32 v21, v21, v14, v15\n\t"
    "v_cvt_pk_bf16_f32 v242, v20, v21\n\t"
    "v_mfma_f32_32x32x16_bf16 v[48:63], v[176:179], a[20:23], v[48:63]\n\t"
    "ds_read_b64_tr_b16 a[8:9], v1 offset:28992\n\t"
    "ds_read_b64_tr_b16 a[10:11], v1 offset:29120\n\t"
    "v_lshlrev_b32 v22, 16, v243\n\t"
    "v_and_b32 v23, s6, v243\n\t"
    "s_waitcnt lgkmcnt(5)\n\t"
    "v_mfma_f32_32x32x16_bf16 v[64:79], a[24:27], v[180:183], v[64:79]\n\t"
    "ds_read_b128 a[12:15], v3 offset:31488\n\t"
    "v_fma_f32 v22, v22, v14, v15\n\t"
    "v_fma_f32 v23, v23, v14, v15\n\t"
    "v_cvt_pk_bf16_f32 v243, v22, v23\n\t"
    "v_mfma_f32_32x32x16_bf16 v[96:111], a[28:31], v[180:183], v[96:111]\n\t"
    "ds_read_b64_tr_b16 a[16:17], v1 offset:27392\n\t"
    "ds_read_b64_tr_b16 a[18:19], v1 offset:27520\n\t"
    "v_lshlrev_b32 v20, 16, v244\n\t"
    "v_and_b32 v21, s6, v244\n\t"
    "s_waitcnt lgkmcnt(5)\n\t"
    "v_mfma_f32_32x32x16_bf16 v[32:47], v[180:183], a[0:3], v[32:47]\n\t"
    "ds_read_b64_tr_b16 a[20:21], v1 offset:29440\n\t"
    "ds_read_b64_tr_b16 a[22:23], v1 offset:29568\n\t"
    "v_fma_f32 v20, v20, v14, v15\n\t"
    "v_fma_f32 v21, v21, v14, v15\n\t"
    "v_cvt_pk_bf16_f32 v244, v20, v21\n\t"
    "v_mfma_f32_32x32x16_bf16 v[80:95], a[4:7], v[180:183], v[80:95]\n\t"
    "ds_read_b128 a[24:27], v2 offset:30528\n\t"
    "v_lshlrev_b32 v22, 16, v245\n\t"
    "v_and_b32 v23, s6, v245\n\t"
    "v_fma_f32 v22, v22, v14, v15\n\t"
    "s_waitcnt lgkmcnt(5)\n\t"
    "v_mfma_f32_32x32x16_bf16 v[112:127], a[8:11], v[180:183], v[112:127]\n\t"
    "ds_read_b64_tr_b16 a[28:29], v1 offset:27456\n\t"
    "ds_read_b64_tr_b16 a[30:31], v1 offset:27584\n\t"
    "v_fma_f32 v23, v23, v14, v15\n\t"
    "v_cvt_pk_bf16_f32 v245, v22, v23\n\t"
    "v_lshlrev_b32 v20, 16, v246\n\t"
    "v_mfma_f32_32x32x16_bf16 v[48:63], v[180:183], a[12:15], v[48:63]\n\t"
    "ds_read_b64_tr_b16 a[0:1], v1 offset:29504\n\t"
    "ds_read_b64_tr_b16 a[2:3], v1 offset:29632\n\t"
    "v_and_b32 v21, s6, v246\n\t"
    "v_fma_f32 v20, v20, v14, v15\n\t"
    "s_waitcnt lgkmcnt(5)\n\t"
    "v_mfma_f32_32x32x16_bf16 v[64:79], a[16:19], v[184:187], v[64:79]\n\t"
    "ds_read_b128 a[4:7], v2 offset:31552\n\t"
    "v_fma_f32 v21, v21, v14, v15\n\t"
    "v_cvt_pk_bf16_f32 v246, v20, v21\n\t"
    "v_lshlrev_b32 v22, 16, v247\n\t"
    "v_mfma_f32_32x32x16_bf16 v[96:111], a[20:23], v[184:187], v[96:111]\n\t"
    "ds_read_b64_tr_b16 a[8:9], v1 offset:27904\n\t"
    "ds_read_b64_tr_b16 a[10:11], v1 offset:28032\n\t"
    "v_and_b32 v23, s6, v247\n\t"
    "v_fma_f32 v22, v22, v14, v15\n\t"
    "s_waitcnt lgkmcnt(5)\n\t"
    "v_mfma_f32_32x32x16_bf16 v[32:47], v[184:187], a[24:27], v[32:47]\n\t"
    "ds_read_b64_tr_b16 a[12:13], v1 offset:29952\n\t"
    "ds_read_b64_tr_b16 a[14:15], v1 offset:30080\n\t"
    "v_fma_f32 v23, v23, v14, v15\n\t"
    "v_cvt_pk_bf16_f32 v247, v22, v23\n\t"
    "v_lshlrev_b32 v20, 16, v248\n\t"
    "v_mfma_f32_32x32x16_bf16 v[80:95], a[28:31], v[184:187], v[80:95]\n\t"
    "ds_read_b128 a[16:19], v3 offset:30528\n\t"
    "v_and_b32 v21, s6, v248\n\t"
    "v_fma_f32 v20, v20, v14, v15\n\t"
    "v_fma_f32 v21, v21, v14, v15\n\t"
    "s_waitcnt lgkmcnt(5)\n\t"
    "v_mfma_f32_32x32x16_bf16 v[112:127], a[0:3], v[184:187], v[112:127]\n\t"
    "ds_read_b64_tr_b16 a[20:21], v1 offset:27968\n\t"
    "ds_read_b64_tr_b16 a[22:23], v1 offset:28096\n\t"
    "s_cmp_lg_u32 s94, 3\n\t"
    "s_cbranch_scc1 Lo36_%=\n\t"
    "s_and_b32 s97, s92, 7\n\t"
    "s_lshl2_add_u32 s97, s97, s95\n\t"
    "s_lshl_b32 s4, s93, 5\n\t"
    "s_add_u32 s4, s4, s97\n\t"
    "s_mul_i32 s4, s4, 14336\n\t"
    "s_add_u32 s4, s4, 3584\n\t"
    "v_min_u32 v17, 432, v0\n\t"
    "v_lshlrev_b32 v17, 3, v17\n\t"
    "buffer_load_dword a53, v17, s[32:35], s4 offen\n\t"
    "Lo36_%=:\n\t"
    "v_cvt_pk_bf16_f32 v248, v20, v21\n\t"
    "v_lshlrev_b32 v22, 16, v249\n\t"
    "v_mfma_f32_32x32x16_bf16 v[48:63], v[184:187], a[4:7], v[48:63]\n\t"
    "ds_read_b64_tr_b16 a[24:25], v1 offset:30016\n\t"
    "ds_read_b64_tr_b16 a[26:27], v1 offset:30144\n\t"
    "v_and_b32 v23, s6, v249\n\t"
    "v_fma_f32 v22, v22, v14, v15\n\t"
    "s_waitcnt lgkmcnt(5)\n\t"
    "v_mfma_f32_32x32x16_bf16 v[64:79], a[8:11], v[188:191], v[64:79]\n\t"
    "ds_read_b128 a[28:31], v3 offset:31552\n\t"
    "v_fma_f32 v23, v23, v14, v15\n\t"
    "v_cvt_pk_bf16_f32 v249, v22, v23\n\t"
    "v_lshlrev_b32 v20, 16, v250\n\t"
    "v_mfma_f32_32x32x16_bf16 v[96:111], a[12:15], v[188:191], v[96:111]\n\t"
    "ds_read_b128 a[0:3], v2 offset:32512\n\t"
    "v_and_b32 v21, s6, v250\n\t"
    "v_fma_f32 v20, v20, v14, v15\n\t"
    "v_fma_f32 v21, v21, v14, v15\n\t"
    "s_waitcnt lgkmcnt(4)\n\t"
    "v_mfma_f32_32x32x16_bf16 v[32:47], v[188:191], a[16:19], v[32:47]\n\t"
    "s_waitcnt vmcnt(6)\n\t"
    "s_barrier\n\t"
    "s_add_u32 m0, s52, 17408\n\t"
    "ds_read_b128 a[4:7], v2 offset:33536\n\t"
    "buffer_load_dwordx4 v8, s[20:23], s65 offen lds\n\t"
    "v_cvt_pk_bf16_f32 v250, v20, v21\n\t"
    "v_lshlrev_b32 v22, 16, v251\n\t"
    "v_mfma_f32_32x32x16_bf16 v[80:95], a[20:23], v[188:191], v[80:95]\n\t"
    "ds_read_b128 a[8:11], v3 offset:32512\n\t"
    "v_and_b32 v23, s6, v251\n\t"
    "v_fma_f32 v22, v22, v14, v15\n\t"
    "v_fma_f32 v23, v23, v14, v15\n\t"
    "s_waitcnt lgkmcnt(3)\n\t"
    "v_mfma_f32_32x32x16_bf16 v[112:127], a[24:27], v[188:191], v[112:127]\n\t"
    "ds_read_b128 a[12:15], v3 offset:33536\n\t"
    "buffer_load_dwordx4 v9, s[20:23], s65 offen offset:1024 lds\n\t"
    "v_cvt_pk_bf16_f32 v251, v22, v23\n\t"
    "v_lshlrev_b32 v20, 16, v252\n\t"
    "v_and_b32 v21, s6, v252\n\t"
    "v_mfma_f32_32x32x16_bf16 v[48:63], v[188:191], a[28:31], v[48:63]\n\t"
    "ds_read_b128 a[16:19], v2 offset:32576\n\t"
    "v_fma_f32 v20, v20, v14, v15\n\t"
    "v_fma_f32 v21, v21, v14, v15\n\t"
    "v_cvt_pk_bf16_f32 v252, v20, v21\n\t"
    "s_waitcnt lgkmcnt(3)\n\t"
    "v_mfma_f32_32x32x16_bf16 v[32:47], v[192:195], a[0:3], v[32:47]\n\t"
    "s_add_u32 m0, s52, 18432\n\t"
    "ds_read_b128 a[20:23], v2 offset:33600\n\t"
    "buffer_load_dwordx4 v8, s[24:27], s65 offen offset:2048 lds\n\t"
    "v_lshlrev_b32 v22, 16, v253\n\t"
    "v_and_b32 v23, s6, v253\n\t"
    "v_mfma_f32_32x32x16_bf16 v[48:63], v[192:195], a[4:7], v[48:63]\n\t"
    "ds_read_b128 a[24:27], v3 offset:32576\n\t"
    "v_fma_f32 v22, v22, v14, v15\n\t"
    "v_fma_f32 v23, v23, v14, v15\n\t"
    "v_cvt_pk_bf16_f32 v253, v22, v23\n\t"
    "s_waitcnt lgkmcnt(3)\n\t"
    "v_mfma_f32_32x32x16_bf16 v[32:47], v[196:199], a[8:11], v[32:47]\n\t"
    "s_add_u32 m0, s52, 19456\n\t"
    "ds_read_b128 a[28:31], v3 offset:33600\n\t"
    "buffer_load_dwordx4 v9, s[24:27], s65 offen offset:3072 lds\n\t"
    "v_lshlrev_b32 v20, 16, v254\n\t"
    "v_and_b32 v21, s6, v254\n\t"
    "v_mfma_f32_32x32x16_bf16 v[48:63], v[196:199], a[12:15], v[48:63]\n\t"
    "ds_read_b128 a[0:3], v2 offset:40704\n\t"
    "v_fma_f32 v20, v20, v14, v15\n\t"
    "v_fma_f32 v21, v21, v14, v15\n\t"
    "v_cvt_pk_bf16_f32 v254, v20, v21\n\t"
    "s_waitcnt lgkmcnt(3)\n\t"
    "v_mfma_f32_32x32x16_bf16 v[32:47], v[200:203], a[16:19], v[32:47]\n\t"
    "ds_read_b128 a[4:7], v2 offset:41728\n\t"
    "s_cmp_lg_u32 s94, 3\n\t"
    "s_cbranch_scc1 Lo37_%=\n\t"
    "s_and_b32 s97, s92, 7\n\t"
    "s_lshl2_add_u32 s97, s97, s95\n\t"
    "s_lshl_b32 s4, s93, 5\n\t"
    "s_add_u32 s4, s4, s97\n\t"
    "s_mul_i32 s4, s4, 14336\n\t"
    "s_add_u32 s4, s4, 7168\n\t"
    "v_min_u32 v17, 432, v0\n\t"
    "v_lshlrev_b32 v17, 3, v17\n\t"
    "buffer_load_dword a53, v17, s[32:35], s4 offen\n\t"
    "Lo37_%=:\n\t"
    "v_lshlrev_b32 v22, 16, v255\n\t"
    "v_and_b32 v23, s6, v255\n\t"
    "v_mfma_f32_32x32x16_bf16 v[48:63], v[200:203], a[20:23], v[48:63]\n\t"
    "ds_read_b128 a[8:11], v3 offset:40704\n\t"
    "v_fma_f32 v22, v22, v14, v15\n\t"
    "v_fma_f32 v23, v23, v14, v15\n\t"
    "v_cvt_pk_bf16_f32 v255, v22, v23\n\t"
    "s_waitcnt lgkmcnt(3)\n\t"
    "v_mfma_f32_32x32x16_bf16 v[32:47], v[204:207], a[24:27], v[32:47]\n\t"
    "s_waitcnt vmcnt(8)\n\t"
    "s_barrier\n\t"
    "s_add_u32 m0, s52, 23552\n\t"
    "ds_read_b128 a[12:15], v3 offset:41728\n\t"
    "buffer_load_dwordx4 v8, s[20:23], s66 offen lds\n\t"
    "v_mfma_f32_32x32x16_bf16 v[48:63], v[204:207], a[28:31], v[48:63]\n\t"
    "ds_read_b128 a[16:19], v2 offset:40768\n\t"
    "s_waitcnt lgkmcnt(3)\n\t"
    "v_mfma_f32_32x32x16_bf16 v[32:47], v[208:211], a[0:3], v[32:47]\n\t"
    "ds_read_b128 a[20:23], v2 offset:41792\n\t"
    "buffer_load_dwordx4 v9, s[20:23], s66 offen offset:1024 lds\n\t"
    "v_mfma_f32_32x32x16_bf16 v[48:63], v[208:211], a[4:7], v[48:63]\n\t"
    "ds_read_b128 a[24:27], v3 offset:40768\n\t"
    "s_waitcnt lgkmcnt(3)\n\t"
    "v_mfma_f32_32x32x16_bf16 v[32:47], v[212:215], a[8:11], v[32:47]\n\t"
    "s_add_u32 m0, s52, 17408\n\t"
    "ds_read_b128 a[28:31], v3 offset:41792\n\t"
    "buffer_load_dwordx4 v8, s[24:27], s66 offen offset:2048 lds\n\t"
    "v_mfma_f32_32x32x16_bf16 v[48:63], v[212:215], a[12:15], v[48:63]\n\t"
    "ds_read_b128 a[0:3], v2 offset:42752\n\t"
    "s_waitcnt lgkmcnt(3)\n\t"
    "v_mfma_f32_32x32x16_bf16 v[32:47], v[216:219], a[16:19], v[32:47]\n\t"
    "s_add_u32 m0, s52, 18432\n\t"
    "ds_read_b128 a[4:7], v2 offset:43776\n\t"
    "buffer_load_dwordx4 v9, s[24:27], s66 offen offset:3072 lds\n\t"
    "v_mfma_f32_32x32x16_bf16 v[48:63], v[216:219], a[20:23], v[48:63]\n\t"
    "ds_read_b128 a[8:11], v3 offset:42752\n\t"
    "s_waitcnt lgkmcnt(3)\n\t"
    "v_mfma_f32_32x32x16_bf16 v[32:47], v[220:223], a[24:27], v[32:47]\n\t"
    "ds_read_b128 a[12:15], v3 offset:43776\n\t"
    "v_mfma_f32_32x32x16_bf16 v[48:63], v[220:223], a[28:31], v[48:63]\n\t"
    "ds_read_b128 a[16:19], v2 offset:42816\n\t"
    "s_waitcnt lgkmcnt(3)\n\t"
    "v_mfma_f32_32x32x16_bf16 v[32:47], v[224:227], a[0:3], v[32:47]\n\t"
    "ds_read_b128 a[20:23], v2 offset:43840\n\t"
    "s_cmp_lg_u32 s94, 3\n\t"
    "s_cbranch_scc1 Lo38_%=\n\t"
    "s_and_b32 s97, s92, 7\n\t"
    "s_lshl2_add_u32 s97, s97, s95\n\t"
    "s_lshl_b32 s4, s93, 5\n\t"
    "s_add_u32 s4, s4, s97\n\t"
    "s_mul_i32 s4, s4, 14336\n\t"
    "s_add_u32 s4, s4, 10752\n\t"
    "v_min_u32 v17, 432, v0\n\t"
    "v_lshlrev_b32 v17, 3, v17\n\t"
    "buffer_load_dword a53, v17, s[32:35], s4 offen\n\t"
    "Lo38_%=:\n\t"
    "v_mfma_f32_32x32x16_bf16 v[48:63], v[224:227], a[4:7], v[48:63]\n\t"
    "ds_read_b128 a[24:27], v3 offset:42816\n\t"
    "s_waitcnt lgkmcnt(3)\n\t"
    "v_mfma_f32_32x32x16_bf16 v[32:47], v[228:231], a[8:11], v[32:47]\n\t"
    "ds_read_b128 a[28:31], v3 offset:43840\n\t"
    "v_mfma_f32_32x32x16_bf16 v[48:63], v[228:231], a[12:15], v[48:63]\n\t"
    "ds_read_b128 a[0:3], v2 offset:34560\n\t"
    "s_waitcnt lgkmcnt(3)\n\t"
    "v_mfma_f32_32x32x16_bf16 v[32:47], v[232:235], a[16:19], v[32:47]\n\t"
    "ds_read_b128 a[4:7], v2 offset:35584\n\t"
    "v_mfma_f32_32x32x16_bf16 v[48:63], v[232:235], a[20:23], v[48:63]\n\t"
    "ds_read_b128 a[8:11], v3 offset:34560\n\t"
    "s_waitcnt lgkmcnt(3)\n\t"
    "v_mfma_f32_32x32x16_bf16 v[32:47], v[236:239], a[24:27], v[32:47]\n\t"
    "s_barrier\n\t"
    "s_add_u32 m0, s52, 32768\n\t"
    "ds_read_b128 a[12:15], v3 offset:35584\n\t"
    "buffer_load_dwordx4 v8, s[20:23], s67 offen lds\n\t"
    "v_mfma_f32_32x32x16_bf16 v[48:63], v[236:239], a[28:31], v[48:63]\n\t"
    "ds_read_b128 a[16:19], v2 offset:34624\n\t"
    "s_waitcnt lgkmcnt(3)\n\t"
    "v_mfma_f32_32x32x16_bf16 v[32:47], v[240:243], a[0:3], v[32:47]\n\t"
    "ds_read_b128 a[20:23], v2 offset:35648\n\t"
    "buffer_load_dwordx4 v9, s[20:23], s67 offen offset:1024 lds\n\t"
    "v_mfma_f32_32x32x16_bf16 v[48:63], v[240:243], a[4:7], v[48:63]\n\t"
    "ds_read_b128 a[24:27], v3 offset:34624\n\t"
    "s_waitcnt lgkmcnt(3)\n\t"
    "v_mfma_f32_32x32x16_bf16 v[32:47], v[244:247], a[8:11], v[32:47]\n\t"
    "ds_read_b128 a[28:31], v3 offset:35648\n\t"
    "buffer_load_dwordx4 v8, s[24:27], s67 offen offset:2048 lds\n\t"
    "v_mfma_f32_32x32x16_bf16 v[48:63], v[244:247], a[12:15], v[48:63]\n\t"
    "ds_read_b64_tr_b16 a[0:1], v1 offset:44800\n\t"
    "ds_read_b64_tr_b16 a[2:3], v1 offset:44928\n\t"
    "s_waitcnt lgkmcnt(4)\n\t"
    "v_mfma_f32_32x32x16_bf16 v[32:47], v[248:251], a[16:19], v[32:47]\n\t"
    "s_add_u32 m0, s52, 22528\n\t"
    "ds_read_b64_tr_b16 a[4:5], v1 offset:7936\n\t"
    "ds_read_b64_tr_b16 a[6:7], v1 offset:8064\n\t"
    "buffer_load_dwordx4 v9, s[24:27], s67 offen offset:3072 lds\n\t"
    "s_cmp_lg_u32 s94, 3\n\t"
    "s_cbranch_scc1 Lo39_%=\n\t"
    "s_and_b32 s97, s92, 7\n\t"
    "s_lshl2_add_u32 s97, s97, s95\n\t"
    "s_lshl_b32 s97, s97, 14\n\t"
    "s_lshl_b32 s4, s93, 8\n\t"
    "s_add_u32 s4, s4, s97\n\t"
    "v_lshrrev_b32 v17, 6, v0\n\t"
    "v_lshlrev_b32 v17, 10, v17\n\t"
    "v_and_b32 v16, 32, v0\n\t"
    "v_lshl_or_b32 v17, v16, 2, v17\n\t"
    "buffer_load_dword a53, v17, s[36:39], s4 offen offset:3072\n\t"
    "Lo39_%=:\n\t"
    "v_mfma_f32_32x32x16_bf16 v[48:63], v[248:251], a[20:23], v[48:63]\n\t"
    "ds_read_b64_tr_b16 a[8:9], v1 offset:44864\n\t"
    "ds_read_b64_tr_b16 a[10:11], v1 offset:44992\n\t"
    "s_waitcnt lgkmcnt(6)\n\t"
    "v_mfma_f32_32x32x16_bf16 v[32:47], v[252:255], a[24:27], v[32:47]\n\t"
    "ds_read_b64_tr_b16 a[12:13], v1 offset:8000\n\t"
    "ds_read_b64_tr_b16 a[14:15], v1 offset:8128\n\t"
    "v_mfma_f32_32x32x16_bf16 v[48:63], v[252:255], a[28:31], v[48:63]\n\t"
    "ds_read_b64_tr_b16 a[16:17], v1 offset:45312\n\t"
    "ds_read_b64_tr_b16 a[18:19], v1 offset:45440\n\t"
    "s_waitcnt lgkmcnt(6)\n\t"
    "v_mfma_f32_32x32x16_bf16 v[64:79], a[0:3], v[192:195], v[64:79]\n\t"
    "ds_read_b64_tr_b16 a[20:21], v1 offset:8448\n\t"
    "ds_read_b64_tr_b16 a[22:23], v1 offset:8576\n\t"
    "v_mfma_f32_32x32x16_bf16 v[96:111], a[4:7], v[192:195], v[96:111]\n\t"
    "ds_read_b64_tr_b16 a[24:25], v1 offset:45376\n\t"
    "ds_read_b64_tr_b16 a[26:27], v1 offset:45504\n\t"
    "v_accvgpr_read_b32 v24, a32\n\t"
    "v_accvgpr_read_b32 v25, a33\n\t"
    "v_accvgpr_read_b32 v27, a44\n\t"
    "v_sub_f32 v26, 0, v25\n\t"
    "s_waitcnt lgkmcnt(6)\n\t"
    "v_mfma_f32_32x32x16_bf16 v[80:95], a[8:11], v[192:195], v[80:95]\n\t"
    "ds_read_b64_tr_b16 a[28:29], v1 offset:8512\n\t"
    "ds_read_b64_tr_b16 a[30:31], v1 offset:8640\n\t"
    "v_fma_f32 v28, v24, v32, v33\n\t"
    "v_fma_f32 v29, v24, v48, v49\n\t"
    "v_fmac_f32 v28, v26, v48\n\t"
    "v_fmac_f32 v29, v25, v32\n\t"
    "v_mfma_f32_32x32x16_bf16 v[112:127], a[12:15], v[192:195], v[112:127]\n\t"
    "ds_read_b64_tr_b16 a[0:1], v1 offset:45824\n\t"
    "ds_read_b64_tr_b16 a[2:3], v1 offset:45952\n\t"
    "v_fma_f32 v20, v24, v28, v34\n\t"
    "v_fma_f32 v21, v24, v29, v50\n\t"
    "v_fmac_f32 v20, v26, v29\n\t"
    "v_fmac_f32 v21, v25, v28\n\t"
    "s_waitcnt lgkmcnt(6)\n\t"
    "v_mfma_f32_32x32x16_bf16 v[64:79], a[16:19], v[196:199], v[64:79]\n\t"
    "ds_read_b64_tr_b16 a[4:5], v1 offset:9984\n\t"
    "ds_read_b64_tr_b16 a[6:7], v1 offset:10112\n\t"
    "v_fma_f32 v28, v24, v20, v35\n\t"
    "v_fma_f32 v29, v24, v21, v51\n\t"
    "v_fmac_f32 v28, v26, v21\n\t"
    "v_fmac_f32 v29, v25, v20\n\t"
    "v_mfma_f32_32x32x16_bf16 v[96:111], a[20:23], v[196:199], v[96:111]\n\t"
    "ds_read_b64_tr_b16 a[8:9], v1 offset:45888\n\t"
    "ds_read_b64_tr_b16 a[10:11], v1 offset:46016\n\t"
    "v_fma_f32 v20, v24, v28, v36\n\t"
    "v_fma_f32 v21, v24, v29, v52\n\t"
    "v_fmac_f32 v20, v26, v29\n\t"
    "v_fmac_f32 v21, v25, v28\n\t"
    "s_waitcnt lgkmcnt(6)\n\t"
    "v_mfma_f32_32x32x16_bf16 v[80:95], a[24:27], v[196:199], v[80:95]\n\t"
    "s_barrier\n\t"
    "s_add_u32 m0, s52, 26624\n\t"
    "ds_read_b64_tr_b16 a[12:13], v1 offset:10048\n\t"
    "ds_read_b64_tr_b16 a[14:15], v1 offset:10176\n\t"
    "buffer_load_dwordx4 v8, s[20:23], s68 offen lds\n\t"
    "v_fma_f32 v28, v24, v20, v37\n\t"
    "v_fma_f32 v29, v24, v21, v53\n\t"
    "Lpad2_%=:\n\t"
    "s_cbranch_execz Lpret_%=\n\t"
    "v_fmac_f32 v28, v26, v21\n\t"
    "v_fmac_f32 v29, v25, v20\n\t"
    "v_mfma_f32_32x32x16_bf16 v[112:127], a[28:31], v[196:199], v[112:127]\n\t"
    "ds_read_b64_tr_b16 a[16:17], v1 offset:46336\n\t"
    "ds_read_b64_tr_b16 a[18:19], v1 offset:46464\n\t"
    "v_fma_f32 v20, v24, v28, v38\n\t"
    "v_fma_f32 v21, v24, v29, v54\n\t"
    "v_fmac_f32 v20, v26, v29\n\t"
    "v_fmac_f32 v21, v25, v28\n\t"
    "s_waitcnt lgkmcnt(6)\n\t"
    "v_mfma_f32_32x32x16_bf16 v[64:79], a[0:3], v[200:203], v[64:79]\n\t"
    "ds_read_b64_tr_b16 a[20:21], v1 offset:10496\n\t"
    "ds_read_b64_tr_b16 a[22:23], v1 offset:10624\n\t"
    "buffer_load_dwordx4 v9, s[20:23], s68 offen offset:1024 lds\n\t"
    "s_cmp_lg_u32 s94, 3\n\t"
    "s_cbranch_scc1 Lo310_%=\n\t"
    "s_and_b32 s97, s92, 7\n\t"
    "s_lshl2_add_u32 s97, s97, s95\n\t"
    "s_lshl_b32 s97, s97, 14\n\t"
    "s_lshl_b32 s4, s93, 8\n\t"
    "s_add_u32 s4, s4, s97\n\t"
    "v_lshrrev_b32 v17, 6, v0\n\t"
    "v_lshlrev_b32 v17, 10, v17\n\t"
    "v_and_b32 v16, 32, v0\n\t"
    "v_lshl_or_b32 v17, v16, 2, v17\n\t"
    "buffer_load_dword a53, v17, s[40:43], s4 offen offset:3072\n\t"
    "Lo310_%=:\n\t"
    "v_fma_f32 v28, v24, v20, v39\n\t"
    "v_fma_f32 v29, v24, v21, v55\n\t"
    "v_fmac_f32 v28, v26, v21\n\t"
    "v_fmac_f32 v29, v25, v20\n\t"
    "v_mfma_f32_32x32x16_bf16 v[96:111], a[4:7], v[200:203], v[96:111]\n\t"
    "ds_read_b64_tr_b16 a[24:25], v1 offset:46400\n\t"
    "ds_read_b64_tr_b16 a[26:27], v1 offset:46528\n\t"
    "v_fma_f32 v20, v24, v28, v40\n\t"
    "v_fma_f32 v21, v24, v29, v56\n\t"
    "v_fmac_f32 v20, v26, v29\n\t"
    "v_fmac_f32 v21, v25, v28\n\t"
    "s_waitcnt lgkmcnt(6)\n\t"
    "v_mfma_f32_32x32x16_bf16 v[80:95], a[8:11], v[200:203], v[80:95]\n\t"
    "s_add_u32 m0, s52, 33792\n\t"
    "ds_read_b64_tr_b16 a[28:29], v1 offset:10560\n\t"
    "ds_read_b64_tr_b16 a[30:31], v1 offset:10688\n\t"
    "buffer_load_dwordx4 v8, s[24:27], s68 offen offset:2048 lds\n\t"
    "v_fma_f32 v28, v24, v20, v41\n\t"
    "v_fma_f32 v29, v24, v21, v57\n\t"
    "v_fmac_f32 v28, v26, v21\n\t"
    "v_fmac_f32 v29, v25, v20\n\t"
    "v_mfma_f32_32x32x16_bf16 v[112:127], a[12:15], v[200:203], v[112:127]\n\t"
    "ds_read_b64_tr_b16 a[0:1], v1 offset:11008\n\t"
    "ds_read_b64_tr_b16 a[2:3], v1 offset:11136\n\t"
    "v_fma_f32 v20, v24, v28, v42\n\t"
    "v_fma_f32 v21, v24, v29, v58\n\t"
    "v_fmac_f32 v20, v26, v29\n\t"
    "v_fmac_f32 v21, v25, v28\n\t"
    "s_waitcnt lgkmcnt(6)\n\t"
    "v_mfma_f32_32x32x16_bf16 v[64:79], a[16:19], v[204:207], v[64:79]\n\t"
    "ds_read_b64_tr_b16 a[4:5], v1 offset:13056\n\t"
    "ds_read_b64_tr_b16 a[6:7], v1 offset:13184\n\t"
    "buffer_load_dwordx4 v9, s[24:27], s68 offen offset:3072 lds\n\t"
    "v_fma_f32 v28, v24, v20, v43\n\t"
    "v_fma_f32 v29, v24, v21, v59\n\t"
    "v_fmac_f32 v28, v26, v21\n\t"
    "v_fmac_f32 v29, v25, v20\n\t"
    "v_mfma_f32_32x32x16_bf16 v[96:111], a[20:23], v[204:207], v[96:111]\n\t"
    "ds_read_b64_tr_b16 a[8:9], v1 offset:11072\n\t"
    "ds_read_b64_tr_b16 a[10:11], v1 offset:11200\n\t"
    "v_fma_f32 v20, v24, v28, v44\n\t"
    "v_fma_f32 v21, v24, v29, v60\n\t"
    "v_fmac_f32 v20, v26, v29\n\t"
    "v_fmac_f32 v21, v25, v28\n\t"
    "s_waitcnt lgkmcnt(6)\n\t"
    "v_mfma_f32_32x32x16_bf16 v[80:95], a[24:27], v[204:207], v[80:95]\n\t"
    "ds_read_b64_tr_b16 a[12:13], v1 offset:13120\n\t"
    "ds_read_b64_tr_b16 a[14:15], v1 offset:13248\n\t"
    "v_fma_f32 v28, v24, v20, v45\n\t"
    "v_fma_f32 v29, v24, v21, v61\n\t"
    "v_fmac_f32 v28, v26, v21\n\t"
    "v_fmac_f32 v29, v25, v20\n\t"
    "v_mfma_f32_32x32x16_bf16 v[112:127], a[28:31], v[204:207], v[112:127]\n\t"
    "ds_read_b64_tr_b16 a[16:17], v1 offset:11520\n\t"
    "ds_read_b64_tr_b16 a[18:19], v1 offset:11648\n\t"
    "v_fma_f32 v20, v24, v28, v46\n\t"
    "v_fma_f32 v21, v24, v29, v62\n\t"
    "v_fmac_f32 v20, v26, v29\n\t"
    "v_fmac_f32 v21, v25, v28\n\t"
    "s_waitcnt lgkmcnt(6)\n\t"
    "v_mfma_f32_32x32x16_bf16 v[64:79], a[0:3], v[208:211], v[64:79]\n\t"
    "ds_read_b64_tr_b16 a[20:21], v1 offset:13568\n\t"
    "ds_read_b64_tr_b16 a[22:23], v1 offset:13696\n\t"
    "v_fma_f32 v28, v24, v20, v47\n\t"
    "v_fma_f32 v29, v24, v21, v63\n\t"
    "v_fmac_f32 v28, v26, v21\n\t"
    "v_fmac_f32 v29, v25, v20\n\t"
    "v_mfma_f32_32x32x16_bf16 v[96:111], a[4:7], v[208:211], v[96:111]\n\t"
    "ds_read_b64_tr_b16 a[24:25], v1 offset:11584\n\t"
    "ds_read_b64_tr_b16 a[26:27], v1 offset:11712\n\t"
    "ds_write_b64 v27, v[28:29] offset:1792\n\t"
    "s_waitcnt lgkmcnt(7)\n\t"
    "v_mfma_f32_32x32x16_bf16 v[80:95], a[8:11], v[208:211], v[80:95]\n\t"
    "ds_read_b64_tr_b16 a[28:29], v1 offset:13632\n\t"
    "ds_read_b64_tr_b16 a[30:31], v1 offset:13760\n\t"
    "v_mfma_f32_32x32x16_bf16 v[112:127], a[12:15], v[208:211], v[112:127]\n\t"
    "ds_read_b64_tr_b16 a[0:1], v1 offset:12032\n\t"
    "ds_read_b64_tr_b16 a[2:3], v1 offset:12160\n\t"
    "s_waitcnt lgkmcnt(4) vmcnt(16)\n\t"
    "s_barrier\n\t"
    "ds_read_b64 v[20:21], v27 offset:0\n\t"
    "ds_read_b64 v[22:23], v27 offset:256\n\t"
    "v_mfma_f32_32x32x16_bf16 v[64:79], a[16:19], v[212:215], v[64:79]\n\t"
    "s_mov_b32 m0, s52\n\t"
    "ds_read_b64_tr_b16 a[4:5], v1 offset:14080\n\t"
    "ds_read_b64_tr_b16 a[6:7], v1 offset:14208\n\t"
    "buffer_load_dwordx4 v8, s[20:23], s69 offen lds\n\t"
    "ds_read_b64 v[4:5], v27 offset:512\n\t"
    "ds_read_b64 v[6:7], v27 offset:768\n\t"
    "v_accvgpr_read_b32 v24, a36\n\t"
    "v_accvgpr_read_b32 v25, a37\n\t"
    "v_mfma_f32_32x32x16_bf16 v[96:111], a[20:23], v[212:215], v[96:111]\n\t"
    "ds_read_b64_tr_b16 a[8:9], v1 offset:12096\n\t"
    "ds_read_b64_tr_b16 a[10:11], v1 offset:12224\n\t"
    "v_sub_f32 v26, 0, v25\n\t"
    "s_waitcnt lgkmcnt(10)\n\t"
    "v_mfma_f32_32x32x16_bf16 v[80:95], a[24:27], v[212:215], v[80:95]\n\t"
    "s_add_u32 m0, s52, 1024\n\t"
    "ds_read_b64_tr_b16 a[12:13], v1 offset:14144\n\t"
    "ds_read_b64_tr_b16 a[14:15], v1 offset:14272\n\t"
    "buffer_load_dwordx4 v9, s[20:23], s69 offen offset:1024 lds\n\t"
    "s_waitcnt lgkmcnt(8)\n\t"
    "v_fmac_f32 v22, v24, v20\n\t"
    "v_fmac_f32 v23, v24, v21\n\t"
    "v_fmac_f32 v22, v26, v21\n\t"
    "v_fmac_f32 v23, v25, v20\n\t"
    "v_mfma_f32_32x32x16_bf16 v[112:127], a[28:31], v[212:215], v[112:127]\n\t"
    "ds_read_b64_tr_b16 a[16:17], v1 offset:12544\n\t"
    "ds_read_b64_tr_b16 a[18:19], v1 offset:12672\n\t"
    "ds_read_b64 v[20:21], v27 offset:1024\n\t"
    "s_waitcnt lgkmcnt(8)\n\t"
    "v_fmac_f32 v4, v24, v22\n\t"
    "v_fmac_f32 v5, v24, v23\n\t"
    "v_fmac_f32 v4, v26, v23\n\t"
    "v_mfma_f32_32x32x16_bf16 v[64:79], a[0:3], v[216:219], v[64:79]\n\t"
    "s_add_u32 m0, s52, 35840\n\t"
    "ds_read_b64_tr_b16 a[20:21], v1 offset:14592\n\t"
    "ds_read_b64_tr_b16 a[22:23], v1 offset:14720\n\t"
    "buffer_load_dwordx4 v8, s[24:27], s69 offen offset:2048 lds\n\t"
    "v_fmac_f32 v5, v25, v22\n\t"
    "ds_read_b64 v[22:23], v27 offset:1280\n\t"
    "s_waitcnt lgkmcnt(10)\n\t"
    "v_fmac_f32 v6, v24, v4\n\t"
    "v_fmac_f32 v7, v24, v5\n\t"
    "v_mfma_f32_32x32x16_bf16 v[96:111], a[4:7], v[216:219], v[96:111]\n\t"
    "ds_read_b64_tr_b16 a[24:25], v1 offset:12608\n\t"
    "ds_read_b64_tr_b16 a[26:27], v1 offset:12736\n\t"
    "v_fmac_f32 v6, v26, v5\n\t"
    "v_fmac_f32 v7, v25, v4\n\t"
    "ds_read_b64 v[4:5], v27 offset:1536\n\t"
    "s_waitcnt lgkmcnt(6)\n\t"
    "v_fmac_f32 v20, v24, v6\n\t"
    "v_mfma_f32_32x32x16_bf16 v[80:95], a[8:11], v[216:219], v[80:95]\n\t"
    "ds_read_b64_tr_b16 a[28:29], v1 offset:14656\n\t"
    "ds_read_b64_tr_b16 a[30:31], v1 offset:14784\n\t"
    "v_fmac_f32 v21, v24, v7\n\t"
    "v_fmac_f32 v20, v26, v7\n\t"
    "v_fmac_f32 v21, v25, v6\n\t"
    "s_waitcnt lgkmcnt(5)\n\t"
    "v_fmac_f32 v22, v24, v20\n\t"
    "v_mfma_f32_32x32x16_bf16 v[112:127], a[12:15], v[216:219], v[112:127]\n\t"
    "ds_read_b64_tr_b16 a[0:1], v1 offset:8960\n\t"
    "ds_read_b64_tr_b16 a[2:3], v1 offset:9088\n\t"
    "v_fmac_f32 v23, v24, v21\n\t"
    "v_fmac_f32 v22, v26, v21\n\t"
    "v_fmac_f32 v23, v25, v20\n\t"
    "s_waitcnt lgkmcnt(4)\n\t"
    "v_fmac_f32 v4, v24, v22\n\t"
    "v_mfma_f32_32x32x16_bf16 v[64:79], a[16:19], v[220:223], v[64:79]\n\t"
    "ds_read_b64_tr_b16 a[4:5], v1 offset:36608\n\t"
    "ds_read_b64_tr_b16 a[6:7], v1 offset:36736\n\t"
    "v_fmac_f32 v5, v24, v23\n\t"
    "v_fmac_f32 v4, v26, v23\n\t"
    "v_fmac_f32 v5, v25, v22\n\t"
    "v_fma_f32 v20, v24, v4, v28\n\t"
    "v_mfma_f32_32x32x16_bf16 v[96:111], a[20:23], v[220:223], v[96:111]\n\t"
    "ds_read_b64_tr_b16 a[8:9], v1 offset:9024\n\t"
    "ds_read_b64_tr_b16 a[10:11], v1 offset:9152\n\t"
    "v_fma_f32 v22, v24, v5, v29\n\t"
    "v_fmac_f32 v20, v26, v5\n\t"
    "v_fmac_f32 v22, v25, v4\n\t"
    "v_mov_b32 v21, s2\n\t"
    "s_waitcnt lgkmcnt(6)\n\t"
    "v_mfma_f32_32x32x16_bf16 v[80:95], a[24:27], v[220:223], v[80:95]\n\t"
    "ds_read_b64_tr_b16 a[12:13], v1 offset:36672\n\t"
    "ds_read_b64_tr_b16 a[14:15], v1 offset:36800\n\t"
    "v_mov_b32 v23, s2\n\t"
    "v_accvgpr_read_b32 v6, a45\n\t"
    "buffer_store_dwordx4 v[20:23], v6, s[44:47], 0 offen sc1\n\t"
    "v_accvgpr_read_b32 v6, a46\n\t"
    "v_mfma_f32_32x32x16_bf16 v[112:127], a[28:31], v[220:223], v[112:127]\n\t"
    "s_barrier\n\t"
    "s_mov_b32 m0, s52\n\t"
    "ds_read_b64_tr_b16 a[16:17], v1 offset:9472\n\t"
    "ds_read_b64_tr_b16 a[18:19], v1 offset:9600\n\t"
    "buffer_load_dwordx4 v9, s[24:27], s69 offen offset:3072 lds\n\t"
    "v_subrev_u32 v7, 131072, v6\n\t"
    "v_accvgpr_read_b32 v24, a34\n\t"
    "s_waitcnt lgkmcnt(6)\n\t"
    "v_mfma_f32_32x32x16_bf16 v[64:79], a[0:3], v[224:227], v[64:79]\n\t"
    "ds_read_b64_tr_b16 a[20:21], v1 offset:37120\n\t"
    "ds_read_b64_tr_b16 a[22:23], v1 offset:37248\n\t"
    "v_accvgpr_read_b32 v25, a35\n\t"
    "v_sub_f32 v26, 0, v24\n\t"
    "v_sub_f32 v25, 0, v25\n\t"
    "v_mfma_f32_32x32x16_bf16 v[96:111], a[4:7], v[224:227], v[96:111]\n\t"
    "s_add_u32 m0, s52, 4096\n\t"
    "ds_read_b64_tr_b16 a[24:25], v1 offset:9536\n\t"
    "ds_read_b64_tr_b16 a[26:27], v1 offset:9664\n\t"
    "buffer_load_dwordx4 v8, s[20:23], s70 offen lds\n\t"
    "v_mul_f32 v28, v25, v32\n\t"
    "v_mul_f32 v32, v24, v32\n\t"
    "s_waitcnt lgkmcnt(6)\n\t"
    "v_mfma_f32_32x32x16_bf16 v[80:95], a[8:11], v[224:227], v[80:95]\n\t"
    "ds_read_b64_tr_b16 a[28:29], v1 offset:37184\n\t"
    "ds_read_b64_tr_b16 a[30:31], v1 offset:37312\n\t"
    "v_fmac_f32 v32, v25, v48\n\t"
    "v_fma_f32 v48, v26, v48, v28\n\t"
    "v_mul_f32 v29, v25, v33\n\t"
    "v_mfma_f32_32x32x16_bf16 v[112:127], a[12:15], v[224:227], v[112:127]\n\t"
    "ds_read_b64_tr_b16 a[0:1], v1 offset:16128\n\t"
    "ds_read_b64_tr_b16 a[2:3], v1 offset:16256\n\t"
    "buffer_load_dwordx4 v9, s[20:23], s70 offen offset:1024 lds\n\t"
    "v_mul_f32 v33, v24, v33\n\t"
    "v_fmac_f32 v33, v25, v49\n\t"
    "s_waitcnt lgkmcnt(6)\n\t"
    "v_mfma_f32_32x32x16_bf16 v[64:79], a[16:19], v[228:231], v[64:79]\n\t"
    "ds_read_b64_tr_b16 a[4:5], v1 offset:15104\n\t"
    "ds_read_b64_tr_b16 a[6:7], v1 offset:15232\n\t"
    "v_fma_f32 v49, v26, v49, v29\n\t"
    "v_mul_f32 v28, v25, v34\n\t"
    "v_mul_f32 v34, v24, v34\n\t"
    "v_mfma_f32_32x32x16_bf16 v[96:111], a[20:23], v[228:231], v[96:111]\n\t"
    "ds_read_b64_tr_b16 a[8:9], v1 offset:16192\n\t"
    "ds_read_b64_tr_b16 a[10:11], v1 offset:16320\n\t"
    "buffer_load_dwordx4 v8, s[24:27], s70 offen offset:2048 lds\n\t"
    "v_fmac_f32 v34, v25, v50\n\t"
    "v_fma_f32 v50, v26, v50, v28\n\t"
    "s_waitcnt lgkmcnt(6)\n\t"
    "v_mfma_f32_32x32x16_bf16 v[80:95], a[24:27], v[228:231], v[80:95]\n\t"
    "ds_read_b64_tr_b16 a[12:13], v1 offset:15168\n\t"
    "ds_read_b64_tr_b16 a[14:15], v1 offset:15296\n\t"
    "v_mul_f32 v29, v25, v35\n\t"
    "v_mul_f32 v35, v24, v35\n\t"
    "v_fmac_f32 v35, v25, v51\n\t"
    "v_mfma_f32_32x32x16_bf16 v[112:127], a[28:31], v[228:231], v[112:127]\n\t"
    "ds_read_b64_tr_b16 a[16:17], v1 offset:16640\n\t"
    "ds_read_b64_tr_b16 a[18:19], v1 offset:16768\n\t"
    "v_fma_f32 v51, v26, v51, v29\n\t"
    "v_mul_f32 v28, v25, v36\n\t"
    "s_waitcnt lgkmcnt(6)\n\t"
    "v_mfma_f32_32x32x16_bf16 v[64:79], a[0:3], v[232:235], v[64:79]\n\t"
    "ds_read_b64_tr_b16 a[20:21], v1 offset:15616\n\t"
    "ds_read_b64_tr_b16 a[22:23], v1 offset:15744\n\t"
    "v_mul_f32 v36, v24, v36\n\t"
    "v_fmac_f32 v36, v25, v52\n\t"
    "v_fma_f32 v52, v26, v52, v28\n\t"
    "v_mfma_f32_32x32x16_bf16 v[96:111], a[4:7], v[232:235], v[96:111]\n\t"
    "ds_read_b64_tr_b16 a[24:25], v1 offset:16704\n\t"
    "ds_read_b64_tr_b16 a[26:27], v1 offset:16832\n\t"
    "v_mul_f32 v29, v25, v37\n\t"
    "v_mul_f32 v37, v24, v37\n\t"
    "s_waitcnt lgkmcnt(6)\n\t"
    "v_mfma_f32_32x32x16_bf16 v[80:95], a[8:11], v[232:235], v[80:95]\n\t"
    "ds_read_b64_tr_b16 a[28:29], v1 offset:15680\n\t"
    "ds_read_b64_tr_b16 a[30:31], v1 offset:15808\n\t"
    "v_fmac_f32 v37, v25, v53\n\t"
    "v_fma_f32 v53, v26, v53, v29\n\t"
    "v_mul_f32 v28, v25, v38\n\t"
    "v_mfma_f32_32x32x16_bf16 v[112:127], a[12:15], v[232:235], v[112:127]\n\t"
    "ds_read_b64_tr_b16 a[0:1], v1 offset:17152\n\t"
    "ds_read_b64_tr_b16 a[2:3], v1 offset:17280\n\t"
    "v_mul_f32 v38, v24, v38\n\t"
    "v_fmac_f32 v38, v25, v54\n\t"
    "s_waitcnt lgkmcnt(6)\n\t"
    "v_mfma_f32_32x32x16_bf16 v[64:79], a[16:19], v[236:239], v[64:79]\n\t"
    "ds_read_b64_tr_b16 a[4:5], v1 offset:19200\n\t"
    "ds_read_b64_tr_b16 a[6:7], v1 offset:19328\n\t"
    "v_fma_f32 v54, v26, v54, v28\n\t"
    "v_mul_f32 v29, v25, v39\n\t"
    "v_mul_f32 v39, v24, v39\n\t"
    "v_mfma_f32_32x32x16_bf16 v[96:111], a[20:23], v[236:239], v[96:111]\n\t"
    "ds_read_b64_tr_b16 a[8:9], v1 offset:17664\n\t"
    "ds_read_b64_tr_b16 a[10:11], v1 offset:17792\n\t"
    "v_fmac_f32 v39, v25, v55\n\t"
    "v_fma_f32 v55, v26, v55, v29\n\t"
    "s_waitcnt lgkmcnt(6)\n\t"
    "v_mfma_f32_32x32x16_bf16 v[80:95], a[24:27], v[236:239], v[80:95]\n\t"
    "ds_read_b64_tr_b16 a[12:13], v1 offset:19712\n\t"
    "ds_read_b64_tr_b16 a[14:15], v1 offset:19840\n\t"
    "v_mul_f32 v28, v25, v40\n\t"
    "v_mul_f32 v40, v24, v40\n\t"
    "v_fmac_f32 v40, v25, v56\n\t"
    "v_mfma_f32_32x32x16_bf16 v[112:127], a[28:31], v[236:239], v[112:127]\n\t"
    "s_waitcnt vmcnt(18)\n\t"
    "s_barrier\n\t"
    "ds_read_b64_tr_b16 a[16:17], v1 offset:18176\n\t"
    "ds_read_b64_tr_b16 a[18:19], v1 offset:18304\n\t"
    "buffer_load_dwordx4 v9, s[24:27], s70 offen offset:3072 lds\n\t"
    "v_fma_f32 v56, v26, v56, v28\n\t"
    "v_mul_f32 v29, v25, v41\n\t"
    "s_waitcnt lgkmcnt(6)\n\t"
    "v_mfma_f32_32x32x16_bf16 v[64:79], a[0:3], v[240:243], v[64:79]\n\t"
    "ds_read_b64_tr_b16 a[20:21], v1 offset:20224\n\t"
    "ds_read_b64_tr_b16 a[22:23], v1 offset:20352\n\t"
    "v_mul_f32 v41, v24, v41\n\t"
    "v_fmac_f32 v41, v25, v57\n\t"
    "v_fma_f32 v57, v26, v57, v29\n\t"
    "v_mfma_f32_32x32x16_bf16 v[96:111], a[4:7], v[240:243], v[96:111]\n\t"
    "s_add_u32 m0, s52, 1024\n\t"
    "ds_read_b64_tr_b16 a[24:25], v1 offset:18688\n\t"
    "ds_read_b64_tr_b16 a[26:27], v1 offset:18816\n\t"
    "buffer_load_dwordx4 v8, s[20:23], s71 offen lds\n\t"
    "v_mul_f32 v28, v25, v42\n\t"
    "v_mul_f32 v42, v24, v42\n\t"
    "s_waitcnt lgkmcnt(6)\n\t"
    "v_mfma_f32_32x32x16_bf16 v[64:79], a[8:11], v[244:247], v[64:79]\n\t"
    "ds_read_b64_tr_b16 a[28:29], v1 offset:20736\n\t"
    "ds_read_b64_tr_b16 a[30:31], v1 offset:20864\n\t"
    "v_fmac_f32 v42, v25, v58\n\t"
    "v_fma_f32 v58, v26, v58, v28\n\t"
    "v_mul_f32 v29, v25, v43\n\t"
    "v_mfma_f32_32x32x16_bf16 v[96:111], a[12:15], v[244:247], v[96:111]\n\t"
    "s_add_u32 m0, s52, 7168\n\t"
    "ds_read_b64_tr_b16 a[0:1], v1 offset:17216\n\t"
    "ds_read_b64_tr_b16 a[2:3], v1 offset:17344\n\t"
    "buffer_load_dwordx4 v9, s[20:23], s71 offen offset:1024 lds\n\t"
    "v_mul_f32 v43, v24, v43\n\t"
    "v_fmac_f32 v43, v25, v59\n\t"
    "s_waitcnt lgkmcnt(6)\n\t"
    "v_mfma_f32_32x32x16_bf16 v[64:79], a[16:19], v[248:251], v[64:79]\n\t"
    "ds_read_b64_tr_b16 a[4:5], v1 offset:19264\n\t"
    "ds_read_b64_tr_b16 a[6:7], v1 offset:19392\n\t"
    "v_fma_f32 v59, v26, v59, v29\n\t"
    "v_mul_f32 v28, v25, v44\n\t"
    "v_mul_f32 v44, v24, v44\n\t"
    "v_mfma_f32_32x32x16_bf16 v[96:111], a[20:23], v[248:251], v[96:111]\n\t"
    "s_add_u32 m0, s52, 26624\n\t"
    "ds_read_b64_tr_b16 a[8:9], v1 offset:17728\n\t"
    "ds_read_b64_tr_b16 a[10:11], v1 offset:17856\n\t"
    "buffer_load_dwordx4 v8, s[24:27], s71 offen offset:2048 lds\n\t"
    "v_fmac_f32 v44, v25, v60\n\t"
    "v_fma_f32 v60, v26, v60, v28\n\t"
    "s_waitcnt lgkmcnt(6)\n\t"
    "v_mfma_f32_32x32x16_bf16 v[64:79], a[24:27], v[252:255], v[64:79]\n\t"
    "ds_read_b64_tr_b16 a[12:13], v1 offset:19776\n\t"
    "ds_read_b64_tr_b16 a[14:15], v1 offset:19904\n\t"
    "v_mul_f32 v29, v25, v45\n\t"
    "v_mul_f32 v45, v24, v45\n\t"
    "v_fmac_f32 v45, v25, v61\n\t"
    "v_mfma_f32_32x32x16_bf16 v[96:111], a[28:31], v[252:255], v[96:111]\n\t"
    "ds_read_b64_tr_b16 a[16:17], v1 offset:18240\n\t"
    "ds_read_b64_tr_b16 a[18:19], v1 offset:18368\n\t"
    "v_fma_f32 v61, v26, v61, v29\n\t"
    "v_mul_f32 v28, v25, v46\n\t"
    "s_waitcnt lgkmcnt(6)\n\t"
    "v_mfma_f32_32x32x16_bf16 v[80:95], a[0:3], v[240:243], v[80:95]\n\t"
    "ds_read_b64_tr_b16 a[20:21], v1 offset:20288\n\t"
    "ds_read_b64_tr_b16 a[22:23], v1 offset:20416\n\t"
    "v_mul_f32 v46, v24, v46\n\t"
    "v_fmac_f32 v46, v25, v62\n\t"
    "v_fma_f32 v62, v26, v62, v28\n\t"
    "v_mfma_f32_32x32x16_bf16 v[112:127], a[4:7], v[240:243], v[112:127]\n\t"
    "ds_read_b64_tr_b16 a[24:25], v1 offset:18752\n\t"
    "ds_read_b64_tr_b16 a[26:27], v1 offset:18880\n\t"
    "v_mul_f32 v64, v64, v96\n\t"
    "v_mul_f32 v65, v65, v97\n\t"
    "v_mul_f32 v66, v66, v98\n\t"
    "v_mul_f32 v67, v67, v99\n\t"
    "v_mul_f32 v68, v68, v100\n\t"
    "s_waitcnt lgkmcnt(6)\n\t"
    "v_mfma_f32_32x32x16_bf16 v[80:95], a[8:11], v[244:247], v[80:95]\n\t"
    "ds_read_b64_tr_b16 a[28:29], v1 offset:20800\n\t"
    "ds_read_b64_tr_b16 a[30:31], v1 offset:20928\n\t"
    "v_mul_f32 v69, v69, v101\n\t"
    "v_mul_f32 v70, v70, v102\n\t"
    "v_mul_f32 v71, v71, v103\n\t"
    "v_cvt_pk_bf16_f32 v96, v64, v65\n\t"
    "v_cvt_pk_bf16_f32 v97, v66, v67\n\t"
    "v_mfma_f32_32x32x16_bf16 v[112:127], a[12:15], v[244:247], v[112:127]\n\t"
    "ds_read_b64_tr_b16 a[0:1], v1 offset:21248\n\t"
    "ds_read_b64_tr_b16 a[2:3], v1 offset:21376\n\t"
    "v_cvt_pk_bf16_f32 v98, v68, v69\n\t"
    "v_cvt_pk_bf16_f32 v99, v70, v71\n\t"
    "v_accvgpr_write_b32 a60, v96\n\t"
    "v_accvgpr_write_b32 a61, v97\n\t"
    "v_accvgpr_write_b32 a62, v98\n\t"
    "s_waitcnt lgkmcnt(6)\n\t"
    "v_mfma_f32_32x32x16_bf16 v[80:95], a[16:19], v[248:251], v[80:95]\n\t"
    "ds_read_b64_tr_b16 a[4:5], v1 offset:23296\n\t"
    "ds_read_b64_tr_b16 a[6:7], v1 offset:23424\n\t"
    "v_accvgpr_write_b32 a63, v99\n\t"
    "v_mul_f32 v72, v72, v104\n\t"
    "v_mul_f32 v73, v73, v105\n\t"
    "v_mul_f32 v74, v74, v106\n\t"
    "v_mul_f32 v75, v75, v107\n\t"
    "v_mfma_f32_32x32x16_bf16 v[112:127], a[20:23], v[248:251], v[112:127]\n\t"
    "ds_read_b64_tr_b16 a[8:9], v1 offset:21760\n\t"
    "ds_read_b64_tr_b16 a[10:11], v1 offset:21888\n\t"
    "v_mul_f32 v76, v76, v108\n\t"
    "v_mul_f32 v77, v77, v109\n\t"
    "v_mul_f32 v78, v78, v110\n\t"
    "v_mul_f32 v79, v79, v111\n\t"
    "v_cvt_pk_bf16_f32 v100, v72, v73\n\t"
    "s_waitcnt lgkmcnt(6)\n\t"
    "v_mfma_f32_32x32x16_bf16 v[80:95], a[24:27], v[252:255], v[80:95]\n\t"
    "ds_read_b64_tr_b16 a[12:13], v1 offset:23808\n\t"
    "ds_read_b64_tr_b16 a[14:15], v1 offset:23936\n\t"
    "v_cvt_pk_bf16_f32 v101, v74, v75\n\t"
    "v_cvt_pk_bf16_f32 v102, v76, v77\n\t"
    "v_cvt_pk_bf16_f32 v103, v78, v79\n\t"
    "v_accvgpr_write_b32 a64, v100\n\t"
    "v_accvgpr_write_b32 a65, v101\n\t"
    "v_mfma_f32_32x32x16_bf16 v[112:127], a[28:31], v[252:255], v[112:127]\n\t"
    "s_waitcnt vmcnt(18)\n\t"
    "s_barrier\n\t"
    "s_add_u32 m0, s52, 6144\n\t"
    "ds_read_b64_tr_b16 a[16:17], v1 offset:22272\n\t"
    "ds_read_b64_tr_b16 a[18:19], v1 offset:22400\n\t"
    "buffer_load_dwordx4 v9, s[24:27], s71 offen offset:3072 lds\n\t"
    "v_accvgpr_write_b32 a66, v102\n\t"
    "v_accvgpr_write_b32 a67, v103\n\t"
    "v_mul_f32 v29, v25, v47\n\t"
    "s_waitcnt lgkmcnt(6)\n\t"
    "v_mfma_f32_32x32x16_bf16 v[64:79], a[0:3], v[128:131], 0\n\t"
    "ds_read_b64_tr_b16 a[20:21], v1 offset:24320\n\t"
    "ds_read_b64_tr_b16 a[22:23], v1 offset:24448\n\t"
    "v_mul_f32 v47, v24, v47\n\t"
    "v_fmac_f32 v47, v25, v63\n\t"
    "v_fma_f32 v63, v26, v63, v29\n\t"
    "v_mfma_f32_32x32x16_bf16 v[96:111], a[4:7], v[128:131], 0\n\t"
    "s_add_u32 m0, s52, 10240\n\t"
    "ds_read_b64_tr_b16 a[24:25], v1 offset:22784\n\t"
    "ds_read_b64_tr_b16 a[26:27], v1 offset:22912\n\t"
    "buffer_load_dwordx4 v10, s[32:35], s72 offen lds\n\t"
    "v_mul_f32 v80, v80, v112\n\t"
    "v_mul_f32 v81, v81, v113\n\t"
    "v_mul_f32 v82, v82, v114\n\t"
    "v_mul_f32 v83, v83, v115\n\t"
    "v_mul_f32 v84, v84, v116\n\t"
    "s_waitcnt lgkmcnt(6)\n\t"
    "v_mfma_f32_32x32x16_bf16 v[64:79], a[8:11], v[132:135], v[64:79]\n\t"
    "ds_read_b64_tr_b16 a[28:29], v1 offset:24832\n\t"
    "ds_read_b64_tr_b16 a[30:31], v1 offset:24960\n\t"
    "v_mul_f32 v85, v85, v117\n\t"
    "v_mul_f32 v86, v86, v118\n\t"
    "v_mul_f32 v87, v87, v119\n\t"
    "v_cvt_pk_bf16_f32 v112, v80, v81\n\t"
    "v_cvt_pk_bf16_f32 v113, v82, v83\n\t"
    "v_mfma_f32_32x32x16_bf16 v[96:111], a[12:15], v[132:135], v[96:111]\n\t"
    "ds_read_b64_tr_b16 a[0:1], v1 offset:21312\n\t"
    "ds_read_b64_tr_b16 a[2:3], v1 offset:21440\n\t"
    "buffer_load_dwordx4 a[248:251], v6, s[44:47], 0 offen sc1\n\t"
    "v_cvt_pk_bf16_f32 v114, v84, v85\n\t"
    "v_cvt_pk_bf16_f32 v115, v86, v87\n\t"
    "v_accvgpr_write_b32 a68, v112\n\t"
    "v_accvgpr_write_b32 a69, v113\n\t"
    "v_accvgpr_write_b32 a70, v114\n\t"
    "s_waitcnt lgkmcnt(6)\n\t"
    "v_mfma_f32_32x32x16_bf16 v[64:79], a[16:19], v[136:139], v[64:79]\n\t"
    "ds_read_b64_tr_b16 a[4:5], v1 offset:23360\n\t"
    "ds_read_b64_tr_b16 a[6:7], v1 offset:23488\n\t"
    "buffer_load_dwordx4 a[252:255], v7, s[44:47], 0 offen sc1\n\t"
    "v_accvgpr_write_b32 a71, v115\n\t"
    "v_mul_f32 v88, v88, v120\n\t"
    "v_mul_f32 v89, v89, v121\n\t"
    "v_mul_f32 v90, v90, v122\n\t"
    "v_mul_f32 v91, v91, v123\n\t"
    "v_mfma_f32_32x32x16_bf16 v[96:111], a[20:23], v[136:139], v[96:111]\n\t"
    "s_add_u32 s5, s72, 15360\n\t"
    "ds_read_b64_tr_b16 a[8:9], v1 offset:21824\n\t"
    "ds_read_b64_tr_b16 a[10:11], v1 offset:21952\n\t"
    "buffer_load_dwordx4 v10, s[32:35], s5 offen offset:1024 lds\n\t"
    "v_mul_f32 v92, v92, v124\n\t"
    "v_mul_f32 v93, v93, v125\n\t"
    "v_mul_f32 v94, v94, v126\n\t"
    "v_mul_f32 v95, v95, v127\n\t"
    "v_cvt_pk_bf16_f32 v116, v88, v89\n\t"
    "s_waitcnt lgkmcnt(6)\n\t"
    "v_mfma_f32_32x32x16_bf16 v[64:79], a[24:27], v[140:143], v[64:79]\n\t"
    "ds_read_b64_tr_b16 a[12:13], v1 offset:23872\n\t"
    "ds_read_b64_tr_b16 a[14:15], v1 offset:24000\n\t"
    "v_cvt_pk_bf16_f32 v117, v90, v91\n\t"
    "v_cvt_pk_bf16_f32 v118, v92, v93\n\t"
    "v_cvt_pk_bf16_f32 v119, v94, v95\n\t"
    "v_accvgpr_write_b32 a72, v116\n\t"
    "v_accvgpr_write_b32 a73, v117\n\t"
    "v_mfma_f32_32x32x16_bf16 v[96:111], a[28:31], v[140:143], v[96:111]\n\t"
    "s_add_u32 s5, s72, 30720\n\t"
    "ds_read_b64_tr_b16 a[16:17], v1 offset:22336\n\t"
    "ds_read_b64_tr_b16 a[18:19], v1 offset:22464\n\t"
    "buffer_load_dwordx4 v10, s[32:35], s5 offen offset:2048 lds\n\t"
    "v_accvgpr_write_b32 a74, v118\n\t"
    "v_accvgpr_write_b32 a75, v119\n\t"
    "v_ashrrev_i32 v24, 31, v6\n\t"
    "s_waitcnt lgkmcnt(6)\n\t"
    "v_mfma_f32_32x32x16_bf16 v[80:95], a[0:3], v[128:131], 0\n\t"
    "ds_read_b64_tr_b16 a[20:21], v1 offset:24384\n\t"
    "ds_read_b64_tr_b16 a[22:23], v1 offset:24512\n\t"
    "v_ashrrev_i32 v25, 31, v7\n\t"
    "v_bfi_b32 v24, v24, 0, s2\n\t"
    "v_bfi_b32 v25, v25, 0, s2\n\t"
    "v_mfma_f32_32x32x16_bf16 v[112:127], a[4:7], v[128:131], 0\n\t"
    "ds_read_b64_tr_b16 a[24:25], v1 offset:22848\n\t"
    "ds_read_b64_tr_b16 a[26:27], v1 offset:22976\n\t"
    "buffer_store_dwordx4 a[60:63], v0, s[44:47], s55 offen sc1\n\t"
    "s_waitcnt lgkmcnt(6)\n\t"
    "v_mfma_f32_32x32x16_bf16 v[80:95], a[8:11], v[132:135], v[80:95]\n\t"
    "ds_read_b64_tr_b16 a[28:29], v1 offset:24896\n\t"
    "ds_read_b64_tr_b16 a[30:31], v1 offset:25024\n\t"
    "buffer_store_dwordx4 a[64:67], v0, s[44:47], s55 offen offset:1024 sc1\n\t"
    "v_mfma_f32_32x32x16_bf16 v[112:127], a[12:15], v[132:135], v[112:127]\n\t"
    "ds_read_b64_tr_b16 a[0:1], v1 offset:25344\n\t"
    "ds_read_b64_tr_b16 a[2:3], v1 offset:25472\n\t"
    "buffer_store_dwordx4 a[68:71], v0, s[44:47], s55 offen offset:2048 sc1\n\t"
    "s_waitcnt lgkmcnt(6)\n\t"
    "v_mfma_f32_32x32x16_bf16 v[80:95], a[16:19], v[136:139], v[80:95]\n\t"
    "ds_read_b64_tr_b16 a[4:5], v1 offset:28416\n\t"
    "ds_read_b64_tr_b16 a[6:7], v1 offset:28544\n\t"
    "buffer_store_dwordx4 a[72:75], v0, s[44:47], s55 offen offset:3072 sc1\n\t"
    "v_mfma_f32_32x32x16_bf16 v[112:127], a[20:23], v[136:139], v[112:127]\n\t"
    "ds_read_b64_tr_b16 a[8:9], v1 offset:25408\n\t"
    "ds_read_b64_tr_b16 a[10:11], v1 offset:25536\n\t"
    "s_waitcnt lgkmcnt(6)\n\t"
    "v_mfma_f32_32x32x16_bf16 v[80:95], a[24:27], v[140:143], v[80:95]\n\t"
    "ds_read_b64_tr_b16 a[12:13], v1 offset:28480\n\t"
    "ds_read_b64_tr_b16 a[14:15], v1 offset:28608\n\t"
    "v_mfma_f32_32x32x16_bf16 v[112:127], a[28:31], v[140:143], v[112:127]\n\t"
    "s_waitcnt vmcnt(24)\n\t"
    "s_barrier\n\t"
    "s_add_u32 s5, s72, 46080\n\t"
    "ds_read_b64_tr_b16 a[16:17], v1 offset:25856\n\t"
    "ds_read_b64_tr_b16 a[18:19], v1 offset:25984\n\t"
    "buffer_load_dwordx4 v10, s[32:35], s5 offen offset:3072 lds\n\t"
    "s_waitcnt lgkmcnt(6)\n\t"
    "v_mfma_f32_32x32x16_bf16 v[64:79], a[0:3], v[144:147], v[64:79]\n\t"
    "ds_read_b64_tr_b16 a[20:21], v1 offset:28928\n\t"
    "ds_read_b64_tr_b16 a[22:23], v1 offset:29056\n\t"
    "v_mfma_f32_32x32x16_bf16 v[96:111], a[4:7], v[144:147], v[96:111]\n\t"
    "s_add_u32 m0, s52, 14336\n\t"
    "ds_read_b64_tr_b16 a[24:25], v1 offset:25920\n\t"
    "ds_read_b64_tr_b16 a[26:27], v1 offset:26048\n\t"
    "buffer_load_dwordx4 v10, s[32:35], s73 offen lds\n\t"
    "s_waitcnt lgkmcnt(6)\n\t"
    "v_mfma_f32_32x32x16_bf16 v[80:95], a[8:11], v[144:147], v[80:95]\n\t"
    "ds_read_b64_tr_b16 a[28:29], v1 offset:28992\n\t"
    "ds_read_b64_tr_b16 a[30:31], v1 offset:29120\n\t"
    "v_mfma_f32_32x32x16_bf16 v[112:127], a[12:15], v[144:147], v[112:127]\n\t"
    "s_add_u32 s5, s73, 15360\n\t"
    "ds_read_b64_tr_b16 a[0:1], v1 offset:26368\n\t"
    "ds_read_b64_tr_b16 a[2:3], v1 offset:26496\n\t"
    "buffer_load_dwordx4 v10, s[32:35], s5 offen offset:1024 lds\n\t"
    "s_waitcnt lgkmcnt(6)\n\t"
    "v_mfma_f32_32x32x16_bf16 v[64:79], a[16:19], v[148:151], v[64:79]\n\t"
    "ds_read_b64_tr_b16 a[4:5], v1 offset:30464\n\t"
    "ds_read_b64_tr_b16 a[6:7], v1 offset:30592\n\t"
    "v_mfma_f32_32x32x16_bf16 v[96:111], a[20:23], v[148:151], v[96:111]\n\t"
    "s_add_u32 s5, s73, 30720\n\t"
    "ds_read_b64_tr_b16 a[8:9], v1 offset:26432\n\t"
    "ds_read_b64_tr_b16 a[10:11], v1 offset:26560\n\t"
    "buffer_load_dwordx4 v10, s[32:35], s5 offen offset:2048 lds\n\t"
    "s_waitcnt lgkmcnt(6)\n\t"
    "v_mfma_f32_32x32x16_bf16 v[80:95], a[24:27], v[148:151], v[80:95]\n\t"
    "ds_read_b64_tr_b16 a[12:13], v1 offset:30528\n\t"
    "ds_read_b64_tr_b16 a[14:15], v1 offset:30656\n\t"
    "v_mfma_f32_32x32x16_bf16 v[112:127], a[28:31], v[148:151], v[112:127]\n\t"
    "ds_read_b64_tr_b16 a[16:17], v1 offset:26880\n\t"
    "ds_read_b64_tr_b16 a[18:19], v1 offset:27008\n\t"
    "s_waitcnt lgkmcnt(6)\n\t"
    "v_mfma_f32_32x32x16_bf16 v[64:79], a[0:3], v[152:155], v[64:79]\n\t"
    "ds_read_b64_tr_b16 a[20:21], v1 offset:30976\n\t"
    "ds_read_b64_tr_b16 a[22:23], v1 offset:31104\n\t"
    "v_mfma_f32_32x32x16_bf16 v[96:111], a[4:7], v[152:155], v[96:111]\n\t"
    "ds_read_b64_tr_b16 a[24:25], v1 offset:26944\n\t"
    "ds_read_b64_tr_b16 a[26:27], v1 offset:27072\n\t"
    "s_waitcnt lgkmcnt(6)\n\t"
    "v_mfma_f32_32x32x16_bf16 v[80:95], a[8:11], v[152:155], v[80:95]\n\t"
    "ds_read_b64_tr_b16 a[28:29], v1 offset:31040\n\t"
    "ds_read_b64_tr_b16 a[30:31], v1 offset:31168\n\t"
    "v_mfma_f32_32x32x16_bf16 v[112:127], a[12:15], v[152:155], v[112:127]\n\t"
    "ds_read_b64_tr_b16 a[0:1], v1 offset:31488\n\t"
    "ds_read_b64_tr_b16 a[2:3], v1 offset:31616\n\t"
    "s_waitcnt lgkmcnt(6)\n\t"
    "v_mfma_f32_32x32x16_bf16 v[64:79], a[16:19], v[156:159], v[64:79]\n\t"
    "ds_read_b64_tr_b16 a[4:5], v1 offset:27392\n\t"
    "ds_read_b64_tr_b16 a[6:7], v1 offset:27520\n\t"
    "v_mfma_f32_32x32x16_bf16 v[96:111], a[20:23], v[156:159], v[96:111]\n\t"
    "ds_read_b64_tr_b16 a[8:9], v1 offset:31552\n\t"
    "ds_read_b64_tr_b16 a[10:11], v1 offset:31680\n\t"
    "s_waitcnt lgkmcnt(6)\n\t"
    "v_mfma_f32_32x32x16_bf16 v[80:95], a[24:27], v[156:159], v[80:95]\n\t"
    "ds_read_b64_tr_b16 a[12:13], v1 offset:27456\n\t"
    "ds_read_b64_tr_b16 a[14:15], v1 offset:27584\n\t"
    "s_waitcnt vmcnt(11)\n\t"
    "v_accvgpr_read_b32 v20, a249\n\t"
    "v_accvgpr_read_b32 v21, a251\n\t"
    "s_waitcnt vmcnt(10)\n\t"
    "v_accvgpr_read_b32 v22, a253\n\t"
    "v_accvgpr_read_b32 v23, a255\n\t"
    "v_xor_b32 v20, v20, v24\n\t"
    "v_xor_b32 v21, v21, v24\n\t"
    "v_xor_b32 v22, v22, v25\n\t"
    "v_xor_b32 v23, v23, v25\n\t"
    "v_or3_b32 v20, v20, v21, v22\n\t"
    "v_or_b32 v20, v20, v23\n\t"
    "v_cmp_ne_u32 vcc, 0, v20\n\t"
    "s_nop 3\n\t"
    "s_cbranch_vccz Lok0_%=\n\t"
    "s_mov_b32 s7, 0x4000\n\t"
    "Lsp0_%=:\n\t"
    "s_sleep 1\n\t"
    "buffer_load_dwordx4 a[248:251], v6, s[44:47], 0 offen sc1\n\t"
    "buffer_load_dwordx4 a[252:255], v7, s[44:47], 0 offen sc1\n\t"
    "Lpad3_%=:\n\t"
    "s_cbranch_execz Lpret_%=\n\t"
    "s_waitcnt vmcnt(0)\n\t"
    "v_accvgpr_read_b32 v20, a249\n\t"
    "v_accvgpr_read_b32 v21, a251\n\t"
    "v_accvgpr_read_b32 v22, a253\n\t"
    "v_accvgpr_read_b32 v23, a255\n\t"
    "v_xor_b32 v20, v20, v24\n\t"
    "v_xor_b32 v21, v21, v24\n\t"
    "v_xor_b32 v22, v22, v25\n\t"
    "v_xor_b32 v23, v23, v25\n\t"
    "v_or3_b32 v20, v20, v21, v22\n\t"
    "v_or_b32 v20, v20, v23\n\t"
    "v_cmp_ne_u32 vcc, 0, v20\n\t"
    "s_nop 4\n\t"
    "s_cbranch_vccz Lok0_%=\n\t"
    "s_sub_u32 s7, s7, 1\n\t"
    "s_cmp_lg_u32 s7, 0\n\t"
    "s_cbranch_scc1 Lsp0_%=\n\t"
    "Lok0_%=:\n\t"
    "v_mfma_f32_32x32x16_bf16 v[112:127], a[28:31], v[156:159], v[112:127]\n\t"
    "s_barrier\n\t"
    "s_add_u32 s5, s73, 46080\n\t"
    "ds_read_b64_tr_b16 a[16:17], v1 offset:32000\n\t"
    "ds_read_b64_tr_b16 a[18:19], v1 offset:32128\n\t"
    "buffer_load_dwordx4 v10, s[32:35], s5 offen offset:3072 lds\n\t"
    "v_accvgpr_read_b32 v20, a248\n\t"
    "v_accvgpr_read_b32 v21, a250\n\t"
    "v_accvgpr_read_b32 v22, a252\n\t"
    "v_accvgpr_read_b32 v23, a254\n\t"
    "s_waitcnt lgkmcnt(6)\n\t"
    "v_mfma_f32_32x32x16_bf16 v[64:79], a[0:3], v[160:163], v[64:79]\n\t"
    "ds_read_b64_tr_b16 a[20:21], v1 offset:27904\n\t"
    "ds_read_b64_tr_b16 a[22:23], v1 offset:28032\n\t"
    "v_accvgpr_read_b32 v24, a42\n\t"
    "v_accvgpr_read_b32 v25, a43\n\t"
    "v_fmac_f32 v20, v24, v22\n\t"
    "v_fmac_f32 v21, v24, v23\n\t"
    "v_mfma_f32_32x32x16_bf16 v[96:111], a[4:7], v[160:163], v[96:111]\n\t"
    "s_add_u32 m0, s52, 18432\n\t"
    "ds_read_b64_tr_b16 a[24:25], v1 offset:32064\n\t"
    "ds_read_b64_tr_b16 a[26:27], v1 offset:32192\n\t"
    "buffer_load_dwordx4 v10, s[32:35], s74 offen lds\n\t"
    "v_fma_f32 v20, -v25, v23, v20\n\t"
    "v_fmac_f32 v21, v25, v22\n\t"
    "v_accvgpr_read_b32 v24, a40\n\t"
    "v_accvgpr_read_b32 v25, a41\n\t"
    "s_waitcnt lgkmcnt(6)\n\t"
    "v_mfma_f32_32x32x16_bf16 v[80:95], a[8:11], v[160:163], v[80:95]\n\t"
    "ds_read_b64_tr_b16 a[28:29], v1 offset:27968\n\t"
    "ds_read_b64_tr_b16 a[30:31], v1 offset:28096\n\t"
    "v_mul_f32 v28, v24, v20\n\t"
    "v_mul_f32 v29, v24, v21\n\t"
    "v_fma_f32 v28, -v25, v21, v28\n\t"
    "v_fmac_f32 v29, v25, v20\n\t"
    "v_mfma_f32_32x32x16_bf16 v[112:127], a[12:15], v[160:163], v[112:127]\n\t"
    "s_add_u32 m0, s52, 19456\n\t"
    "s_add_u32 s5, s74, 15360\n\t"
    "ds_read_b64_tr_b16 a[0:1], v1 offset:32512\n\t"
    "ds_read_b64_tr_b16 a[2:3], v1 offset:32640\n\t"
    "buffer_load_dwordx4 v10, s[32:35], s5 offen offset:1024 lds\n\t"
    "ds_write_b64 v27, v[28:29] offset:3840\n\t"
    "v_lshrrev_b32 v6, 1, v0\n\t"
    "v_and_b32 v6, 0xf8, v6\n\t"
    "s_waitcnt lgkmcnt(7)\n\t"
    "v_mfma_f32_32x32x16_bf16 v[64:79], a[16:19], v[164:167], v[64:79]\n\t"
    "ds_read_b64_tr_b16 a[4:5], v1 offset:29440\n\t"
    "ds_read_b64_tr_b16 a[6:7], v1 offset:29568\n\t"
    "v_mfma_f32_32x32x16_bf16 v[96:111], a[20:23], v[164:167], v[96:111]\n\t"
    "s_add_u32 m0, s52, 20480\n\t"
    "s_add_u32 s5, s74, 30720\n\t"
    "ds_read_b64_tr_b16 a[8:9], v1 offset:32576\n\t"
    "ds_read_b64_tr_b16 a[10:11], v1 offset:32704\n\t"
    "buffer_load_dwordx4 v10, s[32:35], s5 offen offset:2048 lds\n\t"
    "s_waitcnt lgkmcnt(4) vmcnt(12)\n\t"
    "s_barrier\n\t"
    "ds_read_b64 v[20:21], v6 offset:3840\n\t"
    "ds_read_b64 v[22:23], v6 offset:4096\n\t"
    "v_mfma_f32_32x32x16_bf16 v[80:95], a[24:27], v[164:167], v[80:95]\n\t"
    "ds_read_b64_tr_b16 a[12:13], v1 offset:29504\n\t"
    "ds_read_b64_tr_b16 a[14:15], v1 offset:29632\n\t"
    "ds_read_b64 v[24:25], v6 offset:4352\n\t"
    "ds_read_b64 v[26:27], v6 offset:4608\n\t"
    "ds_read_b64 v[28:29], v6 offset:4864\n\t"
    "v_mfma_f32_32x32x16_bf16 v[112:127], a[28:31], v[164:167], v[112:127]\n\t"
    "ds_read_b64_tr_b16 a[16:17], v1 offset:33024\n\t"
    "ds_read_b64_tr_b16 a[18:19], v1 offset:33152\n\t"
    "s_waitcnt lgkmcnt(11)\n\t"
    "v_mfma_f32_32x32x16_bf16 v[64:79], a[0:3], v[168:171], v[64:79]\n\t"
    "ds_read_b64_tr_b16 a[20:21], v1 offset:29952\n\t"
    "ds_read_b64_tr_b16 a[22:23], v1 offset:30080\n\t"
    "s_waitcnt lgkmcnt(9)\n\t"
    "v_add_f32 v20, v20, v22\n\t"
    "v_add_f32 v21, v21, v23\n\t"
    "ds_read_b64 v[22:23], v6 offset:5120\n\t"
    "s_waitcnt lgkmcnt(7)\n\t"
    "v_add_f32 v20, v20, v24\n\t"
    "v_mfma_f32_32x32x16_bf16 v[96:111], a[4:7], v[168:171], v[96:111]\n\t"
    "ds_read_b64_tr_b16 a[24:25], v1 offset:33088\n\t"
    "ds_read_b64_tr_b16 a[26:27], v1 offset:33216\n\t"
    "v_add_f32 v21, v21, v25\n\t"
    "ds_read_b64 v[24:25], v6 offset:5376\n\t"
    "s_waitcnt lgkmcnt(9)\n\t"
    "v_add_f32 v20, v20, v26\n\t"
    "v_add_f32 v21, v21, v27\n\t"
    "v_mfma_f32_32x32x16_bf16 v[80:95], a[8:11], v[168:171], v[80:95]\n\t"
    "ds_read_b64_tr_b16 a[28:29], v1 offset:30016\n\t"
    "ds_read_b64_tr_b16 a[30:31], v1 offset:30144\n\t"
    "ds_read_b64 v[26:27], v6 offset:5632\n\t"
    "s_waitcnt lgkmcnt(11)\n\t"
    "v_add_f32 v20, v20, v28\n\t"
    "v_add_f32 v21, v21, v29\n\t"
    "v_accvgpr_read_b32 v28, a38\n\t"
    "v_mfma_f32_32x32x16_bf16 v[112:127], a[12:15], v[168:171], v[112:127]\n\t"
    "ds_read_b64_tr_b16 a[0:1], v1 offset:40704\n\t"
    "ds_read_b64_tr_b16 a[2:3], v1 offset:40832\n\t"
    "s_waitcnt vmcnt(8)\n\t"
    "s_add_u32 s4, s87, 0\n\t"
    "buffer_store_dwordx4 a[56:59], v18, s[44:47], s4 offen sc1\n\t"
    "v_accvgpr_read_b32 v29, a39\n\t"
    "s_waitcnt lgkmcnt(9)\n\t"
    "v_mfma_f32_32x32x16_bf16 v[64:79], a[16:19], v[172:175], v[64:79]\n\t"
    "ds_read_b64_tr_b16 a[4:5], v1 offset:42752\n\t"
    "ds_read_b64_tr_b16 a[6:7], v1 offset:42880\n\t"
    "s_waitcnt lgkmcnt(10)\n\t"
    "v_add_f32 v20, v20, v22\n\t"
    "v_add_f32 v21, v21, v23\n\t"
    "s_waitcnt lgkmcnt(7)\n\t"
    "v_add_f32 v20, v20, v24\n\t"
    "v_add_f32 v21, v21, v25\n\t"
    "v_mfma_f32_32x32x16_bf16 v[96:111], a[20:23], v[172:175], v[96:111]\n\t"
    "ds_read_b64_tr_b16 a[8:9], v1 offset:40768\n\t"
    "ds_read_b64_tr_b16 a[10:11], v1 offset:40896\n\t"
    "s_waitcnt lgkmcnt(6)\n\t"
    "v_add_f32 v20, v20, v26\n\t"
    "v_add_f32 v21, v21, v27\n\t"
    "v_fmac_f32 v4, v28, v20\n\t"
    "v_fmac_f32 v5, v28, v21\n\t"
    "v_mfma_f32_32x32x16_bf16 v[80:95], a[24:27], v[172:175], v[80:95]\n\t"
    "ds_read_b64_tr_b16 a[12:13], v1 offset:42816\n\t"
    "ds_read_b64_tr_b16 a[14:15], v1 offset:42944\n\t"
    "v_fma_f32 v4, -v29, v21, v4\n\t"
    "v_fmac_f32 v5, v29, v20\n\t"
    "v_accvgpr_read_b32 v22, a34\n\t"
    "v_accvgpr_read_b32 v23, a35\n\t"
    "v_mfma_f32_32x32x16_bf16 v[112:127], a[28:31], v[172:175], v[112:127]\n\t"
    "s_waitcnt vmcnt(7)\n\t"
    "s_barrier\n\t"
    "s_add_u32 s5, s74, 46080\n\t"
    "ds_read_b64_tr_b16 a[16:17], v1 offset:41216\n\t"
    "ds_read_b64_tr_b16 a[18:19], v1 offset:41344\n\t"
    "buffer_load_dwordx4 v10, s[32:35], s5 offen offset:3072 lds\n\t"
    "v_accvgpr_read_b32 v24, a32\n\t"
    "v_accvgpr_read_b32 v26, a33\n\t"
    "v_sub_f32 v25, 0, v26\n\t"
    "v_mul_f32 v28, v22, v4\n\t"
    "s_waitcnt lgkmcnt(6)\n\t"
    "v_mfma_f32_32x32x16_bf16 v[64:79], a[0:3], v[176:179], v[64:79]\n\t"
    "ds_read_b64_tr_b16 a[20:21], v1 offset:43264\n\t"
    "ds_read_b64_tr_b16 a[22:23], v1 offset:43392\n\t"
    "v_mul_f32 v29, v22, v5\n\t"
    "v_fma_f32 v28, -v23, v5, v28\n\t"
    "v_fmac_f32 v29, v23, v4\n\t"
    "v_sub_f32 v29, 0, v29\n\t"
    "v_mfma_f32_32x32x16_bf16 v[96:111], a[4:7], v[176:179], v[96:111]\n\t"
    "s_add_u32 m0, s52, 19456\n\t"
    "ds_read_b64_tr_b16 a[24:25], v1 offset:41280\n\t"
    "ds_read_b64_tr_b16 a[26:27], v1 offset:41408\n\t"
    "buffer_load_dwordx4 v10, s[32:35], s75 offen lds\n\t"
    "v_accvgpr_read_b32 v20, a47\n\t"
    "v_accvgpr_read_b32 v21, a48\n\t"
    "v_fmac_f32 v32, v24, v28\n\t"
    "v_fmac_f32 v48, v24, v29\n\t"
    "s_waitcnt lgkmcnt(6)\n\t"
    "v_mfma_f32_32x32x16_bf16 v[80:95], a[8:11], v[176:179], v[80:95]\n\t"
    "ds_read_b64_tr_b16 a[28:29], v1 offset:43328\n\t"
    "ds_read_b64_tr_b16 a[30:31], v1 offset:43456\n\t"
    "v_fmac_f32 v32, v26, v29\n\t"
    "v_fmac_f32 v48, v25, v28\n\t"
    "v_fmac_f32 v33, v24, v32\n\t"
    "v_fmac_f32 v49, v24, v48\n\t"
    "v_mfma_f32_32x32x16_bf16 v[112:127], a[12:15], v[176:179], v[112:127]\n\t"
    "s_add_u32 m0, s52, 20480\n\t"
    "s_add_u32 s5, s75, 15360\n\t"
    "ds_read_b64_tr_b16 a[0:1], v1 offset:41728\n\t"
    "ds_read_b64_tr_b16 a[2:3], v1 offset:41856\n\t"
    "buffer_load_dwordx4 v10, s[32:35], s5 offen offset:1024 lds\n\t"
    "v_fmac_f32 v33, v26, v48\n\t"
    "v_fmac_f32 v49, v25, v32\n\t"
    "v_cvt_pk_bf16_f32 v32, v32, v33\n\t"
    "v_cvt_pk_bf16_f32 v48, v48, v49\n\t"
    "s_waitcnt lgkmcnt(6)\n\t"
    "v_mfma_f32_32x32x16_bf16 v[64:79], a[16:19], v[180:183], v[64:79]\n\t"
    "ds_read_b64_tr_b16 a[4:5], v1 offset:33536\n\t"
    "ds_read_b64_tr_b16 a[6:7], v1 offset:33664\n\t"
    "v_fmac_f32 v34, v24, v33\n\t"
    "v_fmac_f32 v50, v24, v49\n\t"
    "v_fmac_f32 v34, v26, v49\n\t"
    "v_fmac_f32 v50, v25, v33\n\t"
    "v_mfma_f32_32x32x16_bf16 v[96:111], a[20:23], v[180:183], v[96:111]\n\t"
    "s_add_u32 m0, s52, 22528\n\t"
    "s_add_u32 s5, s75, 30720\n\t"
    "ds_read_b64_tr_b16 a[8:9], v1 offset:41792\n\t"
    "ds_read_b64_tr_b16 a[10:11], v1 offset:41920\n\t"
    "buffer_load_dwordx4 v10, s[32:35], s5 offen offset:2048 lds\n\t"
    "v_fmac_f32 v35, v24, v34\n\t"
    "v_fmac_f32 v51, v24, v50\n\t"
    "v_fmac_f32 v35, v26, v50\n\t"
    "v_fmac_f32 v51, v25, v34\n\t"
    "s_waitcnt lgkmcnt(6)\n\t"
    "v_mfma_f32_32x32x16_bf16 v[80:95], a[24:27], v[180:183], v[80:95]\n\t"
    "ds_read_b64_tr_b16 a[12:13], v1 offset:33600\n\t"
    "ds_read_b64_tr_b16 a[14:15], v1 offset:33728\n\t"
    "v_cvt_pk_bf16_f32 v33, v34, v35\n\t"
    "v_cvt_pk_bf16_f32 v49, v50, v51\n\t"
    "ds_write_b64 v20, v[32:33] offset:0\n\t"
    "v_fmac_f32 v36, v24, v35\n\t"
    "v_mfma_f32_32x32x16_bf16 v[112:127], a[28:31], v[180:183], v[112:127]\n\t"
    "ds_read_b64_tr_b16 a[16:17], v1 offset:42240\n\t"
    "ds_read_b64_tr_b16 a[18:19], v1 offset:42368\n\t"
    "v_fmac_f32 v52, v24, v51\n\t"
    "v_fmac_f32 v36, v26, v51\n\t"
    "v_fmac_f32 v52, v25, v35\n\t"
    "v_fmac_f32 v37, v24, v36\n\t"
    "s_waitcnt lgkmcnt(7)\n\t"
    "v_mfma_f32_32x32x16_bf16 v[64:79], a[0:3], v[184:187], v[64:79]\n\t"
    "ds_read_b64_tr_b16 a[20:21], v1 offset:34048\n\t"
    "ds_read_b64_tr_b16 a[22:23], v1 offset:34176\n\t"
    "v_fmac_f32 v53, v24, v52\n\t"
    "v_fmac_f32 v37, v26, v52\n\t"
    "v_fmac_f32 v53, v25, v36\n\t"
    "v_cvt_pk_bf16_f32 v34, v36, v37\n\t"
    "v_mfma_f32_32x32x16_bf16 v[96:111], a[4:7], v[184:187], v[96:111]\n\t"
    "ds_read_b64_tr_b16 a[24:25], v1 offset:42304\n\t"
    "ds_read_b64_tr_b16 a[26:27], v1 offset:42432\n\t"
    "v_cvt_pk_bf16_f32 v50, v52, v53\n\t"
    "v_fmac_f32 v38, v24, v37\n\t"
    "v_fmac_f32 v54, v24, v53\n\t"
    "v_fmac_f32 v38, v26, v53\n\t"
    "s_waitcnt lgkmcnt(7)\n\t"
    "v_mfma_f32_32x32x16_bf16 v[80:95], a[8:11], v[184:187], v[80:95]\n\t"
    "ds_read_b64_tr_b16 a[28:29], v1 offset:34112\n\t"
    "ds_read_b64_tr_b16 a[30:31], v1 offset:34240\n\t"
    "v_fmac_f32 v54, v25, v37\n\t"
    "v_fmac_f32 v39, v24, v38\n\t"
    "v_fmac_f32 v55, v24, v54\n\t"
    "v_fmac_f32 v39, v26, v54\n\t"
    "v_mfma_f32_32x32x16_bf16 v[112:127], a[12:15], v[184:187], v[112:127]\n\t"
    "ds_read_b64_tr_b16 a[0:1], v1 offset:34560\n\t"
    "ds_read_b64_tr_b16 a[2:3], v1 offset:34688\n\t"
    "v_fmac_f32 v55, v25, v38\n\t"
    "v_cvt_pk_bf16_f32 v35, v38, v39\n\t"
    "v_cvt_pk_bf16_f32 v51, v54, v55\n\t"
    "ds_write_b64 v20, v[34:35] offset:16\n\t"
    "s_waitcnt lgkmcnt(7)\n\t"
    "v_mfma_f32_32x32x16_bf16 v[64:79], a[16:19], v[188:191], v[64:79]\n\t"
    "ds_read_b64_tr_b16 a[4:5], v1 offset:43776\n\t"
    "ds_read_b64_tr_b16 a[6:7], v1 offset:43904\n\t"
    "v_fmac_f32 v40, v24, v39\n\t"
    "v_fmac_f32 v56, v24, v55\n\t"
    "v_fmac_f32 v40, v26, v55\n\t"
    "v_fmac_f32 v56, v25, v39\n\t"
    "v_mfma_f32_32x32x16_bf16 v[96:111], a[20:23], v[188:191], v[96:111]\n\t"
    "ds_read_b64_tr_b16 a[8:9], v1 offset:35072\n\t"
    "ds_read_b64_tr_b16 a[10:11], v1 offset:35200\n\t"
    "v_fmac_f32 v41, v24, v40\n\t"
    "v_fmac_f32 v57, v24, v56\n\t"
    "v_fmac_f32 v41, v26, v56\n\t"
    "v_fmac_f32 v57, v25, v40\n\t"
    "s_waitcnt lgkmcnt(7)\n\t"
    "v_mfma_f32_32x32x16_bf16 v[80:95], a[24:27], v[188:191], v[80:95]\n\t"
    "ds_read_b64_tr_b16 a[12:13], v1 offset:44288\n\t"
    "ds_read_b64_tr_b16 a[14:15], v1 offset:44416\n\t"
    "v_cvt_pk_bf16_f32 v36, v40, v41\n\t"
    "v_cvt_pk_bf16_f32 v52, v56, v57\n\t"
    "v_fmac_f32 v42, v24, v41\n\t"
    "v_fmac_f32 v58, v24, v57\n\t"
    "v_mfma_f32_32x32x16_bf16 v[112:127], a[28:31], v[188:191], v[112:127]\n\t"
    "s_waitcnt vmcnt(7)\n\t"
    "s_barrier\n\t"
    "s_add_u32 s5, s75, 46080\n\t"
    "ds_read_b64_tr_b16 a[16:17], v1 offset:35584\n\t"
    "ds_read_b64_tr_b16 a[18:19], v1 offset:35712\n\t"
    "buffer_load_dwordx4 v10, s[32:35], s5 offen offset:3072 lds\n\t"
    "v_fmac_f32 v42, v26, v57\n\t"
    "v_fmac_f32 v58, v25, v41\n\t"
    "v_fmac_f32 v43, v24, v42\n\t"
    "v_fmac_f32 v59, v24, v58\n\t"
    "s_waitcnt lgkmcnt(6)\n\t"
    "v_mfma_f32_32x32x16_bf16 v[64:79], a[0:3], v[192:195], v[64:79]\n\t"
    "ds_read_b64_tr_b16 a[20:21], v1 offset:44800\n\t"
    "ds_read_b64_tr_b16 a[22:23], v1 offset:44928\n\t"
    "v_fmac_f32 v43, v26, v58\n\t"
    "v_fmac_f32 v59, v25, v42\n\t"
    "v_cvt_pk_bf16_f32 v37, v42, v43\n\t"
    "v_cvt_pk_bf16_f32 v53, v58, v59\n\t"
    "v_mfma_f32_32x32x16_bf16 v[96:111], a[4:7], v[192:195], v[96:111]\n\t"
    "s_add_u32 m0, s52, 31744\n\t"
    "ds_read_b64_tr_b16 a[24:25], v1 offset:36096\n\t"
    "ds_read_b64_tr_b16 a[26:27], v1 offset:36224\n\t"
    "buffer_load_dwordx4 v11, s[36:39], s81 offen offset:1024 lds\n\t"
    "ds_write_b64 v20, v[36:37] offset:32\n\t"
    "v_fmac_f32 v44, v24, v43\n\t"
    "v_fmac_f32 v60, v24, v59\n\t"
    "v_fmac_f32 v44, v26, v59\n\t"
    "s_waitcnt lgkmcnt(7)\n\t"
    "v_mfma_f32_32x32x16_bf16 v[64:79], a[8:11], v[196:199], v[64:79]\n\t"
    "ds_read_b64_tr_b16 a[28:29], v1 offset:45312\n\t"
    "ds_read_b64_tr_b16 a[30:31], v1 offset:45440\n\t"
    "v_fmac_f32 v60, v25, v43\n\t"
    "v_fmac_f32 v45, v24, v44\n\t"
    "v_fmac_f32 v61, v24, v60\n\t"
    "v_fmac_f32 v45, v26, v60\n\t"
    "v_mfma_f32_32x32x16_bf16 v[96:111], a[12:15], v[196:199], v[96:111]\n\t"
    "ds_read_b64_tr_b16 a[0:1], v1 offset:7936\n\t"
    "ds_read_b64_tr_b16 a[2:3], v1 offset:8064\n\t"
    "buffer_load_dwordx4 v11, s[36:39], s82 offen offset:2048 lds\n\t"
    "v_fmac_f32 v61, v25, v44\n\t"
    "v_cvt_pk_bf16_f32 v38, v44, v45\n\t"
    "v_cvt_pk_bf16_f32 v54, v60, v61\n\t"
    "v_fmac_f32 v46, v24, v45\n\t"
    "s_waitcnt lgkmcnt(7)\n\t"
    "v_mfma_f32_32x32x16_bf16 v[64:79], a[16:19], v[200:203], v[64:79]\n\t"
    "ds_read_b64_tr_b16 a[4:5], v1 offset:45824\n\t"
    "ds_read_b64_tr_b16 a[6:7], v1 offset:45952\n\t"
    "v_fmac_f32 v62, v24, v61\n\t"
    "v_fmac_f32 v46, v26, v61\n\t"
    "v_fmac_f32 v62, v25, v45\n\t"
    "v_fmac_f32 v47, v24, v46\n\t"
    "v_mfma_f32_32x32x16_bf16 v[96:111], a[20:23], v[200:203], v[96:111]\n\t"
    "ds_read_b64_tr_b16 a[8:9], v1 offset:8448\n\t"
    "ds_read_b64_tr_b16 a[10:11], v1 offset:8576\n\t"
    "buffer_load_dwordx4 v11, s[36:39], s83 offen offset:3072 lds\n\t"
    "v_fmac_f32 v63, v24, v62\n\t"
    "v_fmac_f32 v47, v26, v62\n\t"
    "v_fmac_f32 v63, v25, v46\n\t"
    "v_cvt_pk_bf16_f32 v39, v46, v47\n\t"
    "s_waitcnt lgkmcnt(6)\n\t"
    "v_mfma_f32_32x32x16_bf16 v[64:79], a[24:27], v[204:207], v[64:79]\n\t"
    "ds_read_b64_tr_b16 a[12:13], v1 offset:46336\n\t"
    "ds_read_b64_tr_b16 a[14:15], v1 offset:46464\n\t"
    "v_cvt_pk_bf16_f32 v55, v62, v63\n\t"
    "ds_write_b64 v20, v[38:39] offset:48\n\t"
    "ds_read_b64_tr_b16 a[76:77], v21 offset:0\n\t"
    "ds_read_b64_tr_b16 a[78:79], v21 offset:288\n\t"
    "v_mfma_f32_32x32x16_bf16 v[96:111], a[28:31], v[204:207], v[96:111]\n\t"
    "ds_read_b64_tr_b16 a[16:17], v1 offset:9984\n\t"
    "ds_read_b64_tr_b16 a[18:19], v1 offset:10112\n\t"
    "ds_read_b64_tr_b16 a[80:81], v21 offset:1152\n\t"
    "ds_read_b64_tr_b16 a[82:83], v21 offset:1440\n\t"
    "s_waitcnt lgkmcnt(14)\n\t"
    "ds_write_b64 v20, v[48:49] offset:0\n\t"
    "s_waitcnt lgkmcnt(14)\n\t"
    "ds_write_b64 v20, v[50:51] offset:16\n\t"
    "s_waitcnt lgkmcnt(13)\n\t"
    "v_mfma_f32_32x32x16_bf16 v[64:79], a[0:3], v[208:211], v[64:79]\n\t"
    "ds_read_b64_tr_b16 a[20:21], v1 offset:11008\n\t"
    "ds_read_b64_tr_b16 a[22:23], v1 offset:11136\n\t"
    "s_waitcnt lgkmcnt(14)\n\t"
    "ds_write_b64 v20, v[52:53] offset:32\n\t"
    "s_waitcnt lgkmcnt(14)\n\t"
    "ds_write_b64 v20, v[54:55] offset:48\n\t"
    "s_waitcnt lgkmcnt(14)\n\t"
    "ds_read_b64_tr_b16 a[84:85], v21 offset:0\n\t"
    "s_waitcnt lgkmcnt(14)\n\t"
    "ds_read_b64_tr_b16 a[86:87], v21 offset:288\n\t"
    "v_mfma_f32_32x32x16_bf16 v[96:111], a[4:7], v[208:211], v[96:111]\n\t"
    "s_waitcnt lgkmcnt(14)\n\t"
    "ds_read_b64_tr_b16 a[24:25], v1 offset:10496\n\t"
    "s_waitcnt lgkmcnt(14)\n\t"
    "ds_read_b64_tr_b16 a[26:27], v1 offset:10624\n\t"
    "s_waitcnt lgkmcnt(14)\n\t"
    "ds_read_b64_tr_b16 a[88:89], v21 offset:1152\n\t"
    "s_waitcnt lgkmcnt(14)\n\t"
    "ds_read_b64_tr_b16 a[90:91], v21 offset:1440\n\t"
    "v_mfma_f32_32x32x16_bf16 v[64:79], a[8:11], v[212:215], v[64:79]\n\t"
    "s_waitcnt lgkmcnt(14)\n\t"
    "ds_read_b64_tr_b16 a[28:29], v1 offset:11520\n\t"
    "s_waitcnt lgkmcnt(14)\n\t"
    "ds_read_b64_tr_b16 a[30:31], v1 offset:11648\n\t"
    "v_mfma_f32_32x32x16_bf16 v[96:111], a[12:15], v[212:215], v[96:111]\n\t"
    "s_waitcnt lgkmcnt(14)\n\t"
    "ds_read_b64_tr_b16 a[0:1], v1 offset:12032\n\t"
    "s_waitcnt lgkmcnt(14)\n\t"
    "ds_read_b64_tr_b16 a[2:3], v1 offset:12160\n\t"
    "s_mov_b32 s4, 1048576\n\t"
    "buffer_load_dwordx2 v[16:17], v19, s[44:47], s4 offen sc1\n\t"
    "s_waitcnt lgkmcnt(4)\n\t"
    "v_mfma_f32_32x32x16_bf16 v[64:79], a[16:19], v[216:219], v[64:79]\n\t"
    "ds_read_b64_tr_b16 a[4:5], v1 offset:14080\n\t"
    "ds_read_b64_tr_b16 a[6:7], v1 offset:14208\n\t"
    "s_add_u32 s4, s55, 8192\n\t"
    "buffer_store_dwordx4 a[76:79], v0, s[44:47], s4 offen sc1\n\t"
    "v_bfe_u32 v24, v0, 5, 4\n\t"
    "v_mfma_f32_32x32x16_bf16 v[96:111], a[20:23], v[216:219], v[96:111]\n\t"
    "ds_read_b64_tr_b16 a[8:9], v1 offset:12544\n\t"
    "ds_read_b64_tr_b16 a[10:11], v1 offset:12672\n\t"
    "s_add_u32 s4, s55, 8192\n\t"
    "buffer_store_dwordx4 a[80:83], v0, s[44:47], s4 offen offset:1024 sc1\n\t"
    "s_waitcnt lgkmcnt(6)\n\t"
    "v_mfma_f32_32x32x16_bf16 v[64:79], a[24:27], v[220:223], v[64:79]\n\t"
    "ds_read_b64_tr_b16 a[12:13], v1 offset:14592\n\t"
    "ds_read_b64_tr_b16 a[14:15], v1 offset:14720\n\t"
    "s_add_u32 s4, s55, 8192\n\t"
    "buffer_store_dwordx4 a[84:87], v0, s[44:47], s4 offen offset:2048 sc1\n\t"
    "v_lshrrev_b32 v25, 9, v0\n\t"
    "v_mfma_f32_32x32x16_bf16 v[96:111], a[28:31], v[220:223], v[96:111]\n\t"
    "ds_read_b64_tr_b16 a[16:17], v1 offset:13056\n\t"
    "ds_read_b64_tr_b16 a[18:19], v1 offset:13184\n\t"
    "s_add_u32 s4, s55, 8192\n\t"
    "buffer_store_dwordx4 a[88:91], v0, s[44:47], s4 offen offset:3072 sc1\n\t"
    "s_waitcnt lgkmcnt(6)\n\t"
    "v_mfma_f32_32x32x16_bf16 v[64:79], a[0:3], v[224:227], v[64:79]\n\t"
    "s_add_u32 m0, s52, 28672\n\t"
    "ds_read_b64_tr_b16 a[20:21], v1 offset:15104\n\t"
    "ds_read_b64_tr_b16 a[22:23], v1 offset:15232\n\t"
    "buffer_load_dwordx4 v11, s[40:43], s81 offen offset:1024 lds\n\t"
    "v_lshlrev_b32 v25, 2, v25\n\t"
    "v_mfma_f32_32x32x16_bf16 v[96:111], a[4:7], v[224:227], v[96:111]\n\t"
    "ds_read_b64_tr_b16 a[24:25], v1 offset:13568\n\t"
    "ds_read_b64_tr_b16 a[26:27], v1 offset:13696\n\t"
    "v_sub_u32 v24, v24, v25\n\t"
    "v_and_b32 v25, 16, v0\n\t"
    "s_waitcnt lgkmcnt(6)\n\t"
    "v_mfma_f32_32x32x16_bf16 v[64:79], a[8:11], v[228:231], v[64:79]\n\t"
    "ds_read_b64_tr_b16 a[28:29], v1 offset:15616\n\t"
    "ds_read_b64_tr_b16 a[30:31], v1 offset:15744\n\t"
    "buffer_load_dwordx4 v11, s[40:43], s82 offen offset:2048 lds\n\t"
    "v_accvgpr_read_b32 v27, a52\n\t"
    "v_lshlrev_b32 v26, v25, s53\n\t"
    "v_mfma_f32_32x32x16_bf16 v[96:111], a[12:15], v[228:231], v[96:111]\n\t"
    "ds_read_b64_tr_b16 a[0:1], v1 offset:8960\n\t"
    "ds_read_b64_tr_b16 a[2:3], v1 offset:9088\n\t"
    "v_lshlrev_b32 v27, v25, v27\n\t"
    "v_cmp_eq_u32 s[0:1], 0, v24\n\t"
    "s_waitcnt lgkmcnt(6)\n\t"
    "v_mfma_f32_32x32x16_bf16 v[64:79], a[16:19], v[232:235], v[64:79]\n\t"
    "ds_read_b64_tr_b16 a[4:5], v1 offset:36608\n\t"
    "ds_read_b64_tr_b16 a[6:7], v1 offset:36736\n\t"
    "buffer_load_dwordx4 v11, s[40:43], s83 offen offset:3072 lds\n\t"
    "v_cndmask_b32 v48, 0, v26, s[0:1]\n\t"
    "v_cndmask_b32 v56, 0, v27, s[0:1]\n\t"
    "v_mfma_f32_32x32x16_bf16 v[96:111], a[20:23], v[232:235], v[96:111]\n\t"
    "ds_read_b64_tr_b16 a[8:9], v1 offset:9472\n\t"
    "ds_read_b64_tr_b16 a[10:11], v1 offset:9600\n\t"
    "v_cmp_eq_u32 s[0:1], 1, v24\n\t"
    "s_waitcnt lgkmcnt(6)\n\t"
    "v_mfma_f32_32x32x16_bf16 v[64:79], a[24:27], v[236:239], v[64:79]\n\t"
    "ds_read_b64_tr_b16 a[12:13], v1 offset:37120\n\t"
    "ds_read_b64_tr_b16 a[14:15], v1 offset:37248\n\t"
    "v_cndmask_b32 v49, 0, v26, s[0:1]\n\t"
    "v_cndmask_b32 v57, 0, v27, s[0:1]\n\t"
    "v_cmp_eq_u32 s[0:1], 2, v24\n\t"
    "v_mfma_f32_32x32x16_bf16 v[96:111], a[28:31], v[236:239], v[96:111]\n\t"
    "ds_read_b64_tr_b16 a[16:17], v1 offset:16128\n\t"
    "ds_read_b64_tr_b16 a[18:19], v1 offset:16256\n\t"
    "v_cndmask_b32 v50, 0, v26, s[0:1]\n\t"
    "v_cndmask_b32 v58, 0, v27, s[0:1]\n\t"
    "s_waitcnt lgkmcnt(6)\n\t"
    "v_mfma_f32_32x32x16_bf16 v[64:79], a[0:3], v[240:243], v[64:79]\n\t"
    "ds_read_b64_tr_b16 a[20:21], v1 offset:17152\n\t"
    "ds_read_b64_tr_b16 a[22:23], v1 offset:17280\n\t"
    "v_cmp_eq_u32 s[0:1], 3, v24\n\t"
    "v_mfma_f32_32x32x16_bf16 v[96:111], a[4:7], v[240:243], v[96:111]\n\t"
    "ds_read_b64_tr_b16 a[24:25], v1 offset:16640\n\t"
    "ds_read_b64_tr_b16 a[26:27], v1 offset:16768\n\t"
    "v_cndmask_b32 v51, 0, v26, s[0:1]\n\t"
    "v_cndmask_b32 v59, 0, v27, s[0:1]\n\t"
    "s_waitcnt lgkmcnt(6)\n\t"
    "v_mfma_f32_32x32x16_bf16 v[64:79], a[8:11], v[244:247], v[64:79]\n\t"
    "ds_read_b64_tr_b16 a[28:29], v1 offset:17664\n\t"
    "ds_read_b64_tr_b16 a[30:31], v1 offset:17792\n\t"
    "v_cmp_eq_u32 s[0:1], 8, v24\n\t"
    "v_mfma_f32_32x32x16_bf16 v[96:111], a[12:15], v[244:247], v[96:111]\n\t"
    "ds_read_b64_tr_b16 a[0:1], v1 offset:34624\n\t"
    "ds_read_b64_tr_b16 a[2:3], v1 offset:34752\n\t"
    "v_cndmask_b32 v52, 0, v26, s[0:1]\n\t"
    "v_cndmask_b32 v60, 0, v27, s[0:1]\n\t"
    "s_waitcnt lgkmcnt(6)\n\t"
    "v_mfma_f32_32x32x16_bf16 v[64:79], a[16:19], v[248:251], v[64:79]\n\t"
    "ds_read_b64_tr_b16 a[4:5], v1 offset:43840\n\t"
    "ds_read_b64_tr_b16 a[6:7], v1 offset:43968\n\t"
    "v_cmp_eq_u32 s[0:1], 9, v24\n\t"
    "v_mfma_f32_32x32x16_bf16 v[96:111], a[20:23], v[248:251], v[96:111]\n\t"
    "ds_read_b64_tr_b16 a[8:9], v1 offset:35136\n\t"
    "ds_read_b64_tr_b16 a[10:11], v1 offset:35264\n\t"
    "v_cndmask_b32 v53, 0, v26, s[0:1]\n\t"
    "v_cndmask_b32 v61, 0, v27, s[0:1]\n\t"
    "s_waitcnt lgkmcnt(6)\n\t"
    "v_mfma_f32_32x32x16_bf16 v[64:79], a[24:27], v[252:255], v[64:79]\n\t"
    "ds_read_b64_tr_b16 a[12:13], v1 offset:44352\n\t"
    "ds_read_b64_tr_b16 a[14:15], v1 offset:44480\n\t"
    "v_cmp_eq_u32 s[0:1], 10, v24\n\t"
    "v_mfma_f32_32x32x16_bf16 v[96:111], a[28:31], v[252:255], v[96:111]\n\t"
    "ds_read_b64_tr_b16 a[16:17], v1 offset:35648\n\t"
    "ds_read_b64_tr_b16 a[18:19], v1 offset:35776\n\t"
    "v_cndmask_b32 v54, 0, v26, s[0:1]\n\t"
    "v_cndmask_b32 v62, 0, v27, s[0:1]\n\t"
    "s_waitcnt lgkmcnt(6)\n\t"
    "v_mfma_f32_32x32x16_bf16 v[80:95], a[0:3], v[192:195], v[80:95]\n\t"
    "ds_read_b64_tr_b16 a[20:21], v1 offset:44864\n\t"
    "ds_read_b64_tr_b16 a[22:23], v1 offset:44992\n\t"
    "v_cmp_eq_u32 s[0:1], 11, v24\n\t"
    "v_mfma_f32_32x32x16_bf16 v[112:127], a[4:7], v[192:195], v[112:127]\n\t"
    "ds_read_b64_tr_b16 a[24:25], v1 offset:36160\n\t"
    "ds_read_b64_tr_b16 a[26:27], v1 offset:36288\n\t"
    "v_mul_f32 v64, v64, v96\n\t"
    "v_mul_f32 v65, v65, v97\n\t"
    "s_waitcnt lgkmcnt(6)\n\t"
    "v_mfma_f32_32x32x16_bf16 v[80:95], a[8:11], v[196:199], v[80:95]\n\t"
    "ds_read_b64_tr_b16 a[28:29], v1 offset:45376\n\t"
    "ds_read_b64_tr_b16 a[30:31], v1 offset:45504\n\t"
    "v_mul_f32 v66, v66, v98\n\t"
    "v_mul_f32 v67, v67, v99\n\t"
    "v_mul_f32 v68, v68, v100\n\t"
    "v_mfma_f32_32x32x16_bf16 v[112:127], a[12:15], v[196:199], v[112:127]\n\t"
    "ds_read_b64_tr_b16 a[0:1], v1 offset:8000\n\t"
    "ds_read_b64_tr_b16 a[2:3], v1 offset:8128\n\t"
    "v_mul_f32 v69, v69, v101\n\t"
    "v_mul_f32 v70, v70, v102\n\t"
    "s_waitcnt lgkmcnt(6)\n\t"
    "v_mfma_f32_32x32x16_bf16 v[80:95], a[16:19], v[200:203], v[80:95]\n\t"
    "ds_read_b64_tr_b16 a[4:5], v1 offset:45888\n\t"
    "ds_read_b64_tr_b16 a[6:7], v1 offset:46016\n\t"
    "v_mul_f32 v71, v71, v103\n\t"
    "v_cvt_pk_bf16_f32 v64, v64, v65\n\t"
    "v_cvt_pk_bf16_f32 v65, v66, v67\n\t"
    "v_mfma_f32_32x32x16_bf16 v[112:127], a[20:23], v[200:203], v[112:127]\n\t"
    "ds_read_b64_tr_b16 a[8:9], v1 offset:8512\n\t"
    "ds_read_b64_tr_b16 a[10:11], v1 offset:8640\n\t"
    "v_cvt_pk_bf16_f32 v66, v68, v69\n\t"
    "v_cvt_pk_bf16_f32 v67, v70, v71\n\t"
    "s_waitcnt lgkmcnt(6)\n\t"
    "v_mfma_f32_32x32x16_bf16 v[80:95], a[24:27], v[204:207], v[80:95]\n\t"
    "ds_read_b64_tr_b16 a[12:13], v1 offset:46400\n\t"
    "ds_read_b64_tr_b16 a[14:15], v1 offset:46528\n\t"
    "v_mul_f32 v72, v72, v104\n\t"
    "v_mul_f32 v73, v73, v105\n\t"
    "v_mul_f32 v74, v74, v106\n\t"
    "v_mfma_f32_32x32x16_bf16 v[112:127], a[28:31], v[204:207], v[112:127]\n\t"
    "s_waitcnt vmcnt(8)\n\t"
    "s_barrier\n\t"
    "s_add_u32 m0, s52, 25600\n\t"
    "ds_read_b64_tr_b16 a[16:17], v1 offset:10048\n\t"
    "ds_read_b64_tr_b16 a[18:19], v1 offset:10176\n\t"
    "buffer_load_dwordx4 v12, s[36:39], s81 offen offset:1024 lds\n\t"
    "v_mul_f32 v75, v75, v107\n\t"
    "v_mul_f32 v76, v76, v108\n\t"
    "s_waitcnt lgkmcnt(6)\n\t"
    "v_mfma_f32_32x32x16_bf16 v[80:95], a[0:3], v[208:211], v[80:95]\n\t"
    "ds_read_b64_tr_b16 a[20:21], v1 offset:11072\n\t"
    "ds_read_b64_tr_b16 a[22:23], v1 offset:11200\n\t"
    "s_waitcnt vmcnt(8)\n\t"
    "v_cmp_ne_u64 vcc, s[2:3], v[16:17]\n\t"
    "s_nop 3\n\t"
    "s_cbranch_vccz Lok1_%=\n\t"
    "s_mov_b32 s7, 0x4000\n\t"
    "Lsp1_%=:\n\t"
    "s_sleep 1\n\t"
    "s_mov_b32 s4, 1048576\n\t"
    "buffer_load_dwordx2 v[16:17], v19, s[44:47], s4 offen sc1\n\t"
    "s_waitcnt vmcnt(0)\n\t"
    "v_cmp_ne_u64 vcc, s[2:3], v[16:17]\n\t"
    "s_nop 4\n\t"
    "s_cbranch_vccz Lok1_%=\n\t"
    "s_sub_u32 s7, s7, 1\n\t"
    "s_cmp_lg_u32 s7, 0\n\t"
    "s_cbranch_scc1 Lsp1_%=\n\t"
    "Lok1_%=:\n\t"
    "v_mul_f32 v77, v77, v109\n\t"
    "v_mul_f32 v78, v78, v110\n\t"
    "v_mfma_f32_32x32x16_bf16 v[112:127], a[4:7], v[208:211], v[112:127]\n\t"
    "ds_read_b64_tr_b16 a[24:25], v1 offset:10560\n\t"
    "ds_read_b64_tr_b16 a[26:27], v1 offset:10688\n\t"
    "buffer_load_dwordx4 a[92:95], v0, s[44:47], s84 offen sc1\n\t"
    "v_mul_f32 v79, v79, v111\n\t"
    "v_cvt_pk_bf16_f32 v68, v72, v73\n\t"
    "s_waitcnt lgkmcnt(6)\n\t"
    "v_mfma_f32_32x32x16_bf16 v[80:95], a[8:11], v[212:215], v[80:95]\n\t"
    "ds_read_b64_tr_b16 a[28:29], v1 offset:11584\n\t"
    "ds_read_b64_tr_b16 a[30:31], v1 offset:11712\n\t"
    "v_cvt_pk_bf16_f32 v69, v74, v75\n\t"
    "v_cvt_pk_bf16_f32 v70, v76, v77\n\t"
    "v_cvt_pk_bf16_f32 v71, v78, v79\n\t"
    "v_mfma_f32_32x32x16_bf16 v[112:127], a[12:15], v[212:215], v[112:127]\n\t"
    "ds_read_b64_tr_b16 a[0:1], v1 offset:12096\n\t"
    "ds_read_b64_tr_b16 a[2:3], v1 offset:12224\n\t"
    "s_add_u32 s4, s55, 4096\n\t"
    "buffer_store_dwordx4 v[64:67], v30, s[44:47], s4 offen sc1\n\t"
    "s_waitcnt lgkmcnt(6)\n\t"
    "v_mfma_f32_32x32x16_bf16 v[80:95], a[16:19], v[216:219], v[80:95]\n\t"
    "ds_read_b64_tr_b16 a[4:5], v1 offset:14144\n\t"
    "ds_read_b64_tr_b16 a[6:7], v1 offset:14272\n\t"
    "s_add_u32 s4, s55, 4096\n\t"
    "buffer_store_dwordx4 v[68:71], v30, s[44:47], s4 offen offset:1024 sc1\n\t"
    "v_cndmask_b32 v55, 0, v26, s[0:1]\n\t"
    "v_mfma_f32_32x32x16_bf16 v[112:127], a[20:23], v[216:219], v[112:127]\n\t"
    "ds_read_b64_tr_b16 a[8:9], v1 offset:12608\n\t"
    "ds_read_b64_tr_b16 a[10:11], v1 offset:12736\n\t"
    "buffer_load_dwordx4 a[96:99], v0, s[44:47], s84 offen offset:1024 sc1\n\t"
    "v_cndmask_b32 v63, 0, v27, s[0:1]\n\t"
    "s_waitcnt lgkmcnt(6)\n\t"
    "v_mfma_f32_32x32x16_bf16 v[80:95], a[24:27], v[220:223], v[80:95]\n\t"
    "ds_read_b64_tr_b16 a[12:13], v1 offset:14656\n\t"
    "ds_read_b64_tr_b16 a[14:15], v1 offset:14784\n\t"
    "v_accvgpr_read_b32 v32, a220\n\t"
    "v_accvgpr_read_b32 v33, a221\n\t"
    "v_accvgpr_read_b32 v34, a222\n\t"
    "v_mfma_f32_32x32x16_bf16 v[112:127], a[28:31], v[220:223], v[112:127]\n\t"
    "ds_read_b64_tr_b16 a[16:17], v1 offset:13120\n\t"
    "ds_read_b64_tr_b16 a[18:19], v1 offset:13248\n\t"
    "buffer_load_dwordx4 a[100:103], v0, s[44:47], s84 offen offset:2048 sc1\n\t"
    "v_accvgpr_read_b32 v35, a223\n\t"
    "s_waitcnt lgkmcnt(6)\n\t"
    "v_mfma_f32_32x32x16_bf16 v[80:95], a[0:3], v[224:227], v[80:95]\n\t"
    "ds_read_b64_tr_b16 a[20:21], v1 offset:15168\n\t"
    "ds_read_b64_tr_b16 a[22:23], v1 offset:15296\n\t"
    "v_accvgpr_read_b32 v36, a224\n\t"
    "v_accvgpr_read_b32 v37, a225\n\t"
    "v_accvgpr_read_b32 v38, a226\n\t"
    "Lpad4_%=:\n\t"
    "s_cbranch_execz Lpret_%=\n\t"
    "v_mfma_f32_32x32x16_bf16 v[112:127], a[4:7], v[224:227], v[112:127]\n\t"
    "ds_read_b64_tr_b16 a[24:25], v1 offset:13632\n\t"
    "ds_read_b64_tr_b16 a[26:27], v1 offset:13760\n\t"
    "buffer_load_dwordx4 a[104:107], v0, s[44:47], s84 offen offset:3072 sc1\n\t"
    "v_accvgpr_read_b32 v39, a227\n\t"
    "s_waitcnt lgkmcnt(6)\n\t"
    "v_mfma_f32_32x32x16_bf16 v[80:95], a[8:11], v[228:231], v[80:95]\n\t"
    "ds_read_b64_tr_b16 a[28:29], v1 offset:15680\n\t"
    "ds_read_b64_tr_b16 a[30:31], v1 offset:15808\n\t"
    "s_waitcnt vmcnt(10)\n\t"
    "v_lshlrev_b32 v20, 16, v32\n\t"
    "v_and_b32 v21, s6, v32\n\t"
    "v_mfma_f32_32x32x16_bf16 v[112:127], a[12:15], v[228:231], v[112:127]\n\t"
    "ds_read_b64_tr_b16 a[0:1], v1 offset:9024\n\t"
    "ds_read_b64_tr_b16 a[2:3], v1 offset:9152\n\t"
    "s_add_u32 s4, s87, 131072\n\t"
    "buffer_store_dwordx4 a[56:59], v18, s[44:47], s4 offen sc1\n\t"
    "s_waitcnt lgkmcnt(6)\n\t"
    "v_mfma_f32_32x32x16_bf16 v[80:95], a[16:19], v[232:235], v[80:95]\n\t"
    "ds_read_b64_tr_b16 a[4:5], v1 offset:36672\n\t"
    "ds_read_b64_tr_b16 a[6:7], v1 offset:36800\n\t"
    "buffer_load_dwordx4 a[108:111], v0, s[44:47], s85 offen sc1\n\t"
    "v_fma_f32 v20, v20, v14, v15\n\t"
    "v_fma_f32 v21, v21, v14, v15\n\t"
    "v_mfma_f32_32x32x16_bf16 v[112:127], a[20:23], v[232:235], v[112:127]\n\t"
    "ds_read_b64_tr_b16 a[8:9], v1 offset:9536\n\t"
    "ds_read_b64_tr_b16 a[10:11], v1 offset:9664\n\t"
    "v_cvt_pk_bf16_f32 v40, v20, v21\n\t"
    "v_lshlrev_b32 v22, 16, v33\n\t"
    "s_waitcnt lgkmcnt(6)\n\t"
    "v_mfma_f32_32x32x16_bf16 v[80:95], a[24:27], v[236:239], v[80:95]\n\t"
    "ds_read_b64_tr_b16 a[12:13], v1 offset:37184\n\t"
    "ds_read_b64_tr_b16 a[14:15], v1 offset:37312\n\t"
    "buffer_load_dwordx4 a[112:115], v0, s[44:47], s85 offen offset:1024 sc1\n\t"
    "v_and_b32 v23, s6, v33\n\t"
    "v_fma_f32 v22, v22, v14, v15\n\t"
    "v_mfma_f32_32x32x16_bf16 v[112:127], a[28:31], v[236:239], v[112:127]\n\t"
    "ds_read_b64_tr_b16 a[16:17], v1 offset:16192\n\t"
    "ds_read_b64_tr_b16 a[18:19], v1 offset:16320\n\t"
    "v_fma_f32 v23, v23, v14, v15\n\t"
    "v_cvt_pk_bf16_f32 v41, v22, v23\n\t"
    "s_waitcnt lgkmcnt(6)\n\t"
    "v_mfma_f32_32x32x16_bf16 v[80:95], a[0:3], v[240:243], v[80:95]\n\t"
    "ds_read_b64_tr_b16 a[20:21], v1 offset:17216\n\t"
    "ds_read_b64_tr_b16 a[22:23], v1 offset:17344\n\t"
    "buffer_load_dwordx4 a[116:119], v0, s[44:47], s85 offen offset:2048 sc1\n\t"
    "v_lshlrev_b32 v20, 16, v34\n\t"
    "v_and_b32 v21, s6, v34\n\t"
    "v_mfma_f32_32x32x16_bf16 v[112:127], a[4:7], v[240:243], v[112:127]\n\t"
    "ds_read_b64_tr_b16 a[24:25], v1 offset:16704\n\t"
    "ds_read_b64_tr_b16 a[26:27], v1 offset:16832\n\t"
    "v_fma_f32 v20, v20, v14, v15\n\t"
    "v_fma_f32 v21, v21, v14, v15\n\t"
    "s_waitcnt lgkmcnt(6)\n\t"
    "v_mfma_f32_32x32x16_bf16 v[80:95], a[8:11], v[244:247], v[80:95]\n\t"
    "ds_read_b64_tr_b16 a[28:29], v1 offset:17728\n\t"
    "ds_read_b64_tr_b16 a[30:31], v1 offset:17856\n\t"
    "buffer_load_dwordx4 a[120:123], v0, s[44:47], s85 offen offset:3072 sc1\n\t"
    "v_cvt_pk_bf16_f32 v42, v20, v21\n\t"
    "v_lshlrev_b32 v22, 16, v35\n\t"
    "v_mfma_f32_32x32x16_bf16 v[112:127], a[12:15], v[244:247], v[112:127]\n\t"
    "ds_read_b64_tr_b16 a[0:1], v1 offset:18240\n\t"
    "ds_read_b64_tr_b16 a[2:3], v1 offset:18752\n\t"
    "v_and_b32 v23, s6, v35\n\t"
    "v_fma_f32 v22, v22, v14, v15\n\t"
    "s_waitcnt lgkmcnt(6)\n\t"
    "v_mfma_f32_32x32x16_bf16 v[80:95], a[16:19], v[248:251], v[80:95]\n\t"
    "ds_read_b64_tr_b16 a[4:5], v1 offset:18304\n\t"
    "ds_read_b64_tr_b16 a[6:7], v1 offset:18816\n\t"
    "buffer_load_dwordx4 a[124:127], v0, s[44:47], s86 offen sc1\n\t"
    "v_fma_f32 v23, v23, v14, v15\n\t"
    "v_cvt_pk_bf16_f32 v43, v22, v23\n\t"
    "v_mfma_f32_32x32x16_bf16 v[112:127], a[20:23], v[248:251], v[112:127]\n\t"
    "ds_read_b64_tr_b16 a[8:9], v1 offset:18368\n\t"
    "ds_read_b64_tr_b16 a[10:11], v1 offset:18880\n\t"
    "v_lshlrev_b32 v20, 16, v36\n\t"
    "v_and_b32 v21, s6, v36\n\t"
    "s_waitcnt lgkmcnt(6)\n\t"
    "v_mfma_f32_32x32x16_bf16 v[80:95], a[24:27], v[252:255], v[80:95]\n\t"
    "ds_read_b64_tr_b16 a[12:13], v1 offset:18176\n\t"
    "ds_read_b64_tr_b16 a[14:15], v1 offset:18688\n\t"
    "buffer_load_dwordx4 a[128:131], v0, s[44:47], s86 offen offset:1024 sc1\n\t"
    "v_fma_f32 v20, v20, v14, v15\n\t"
    "v_fma_f32 v21, v21, v14, v15\n\t"
    "v_mfma_f32_32x32x16_bf16 v[112:127], a[28:31], v[252:255], v[112:127]\n\t"
    "ds_read_b64_tr_b16 a[16:17], v1 offset:19264\n\t"
    "ds_read_b64_tr_b16 a[18:19], v1 offset:19776\n\t"
    "v_cvt_pk_bf16_f32 v44, v20, v21\n\t"
    "v_lshlrev_b32 v22, 16, v37\n\t"
    "s_waitcnt lgkmcnt(6)\n\t"
    "v_mfma_f32_32x32x16_bf16 v[144:159], a[60:63], a[0:3], 0\n\t"
    "ds_read_b64_tr_b16 a[20:21], v1 offset:19328\n\t"
    "ds_read_b64_tr_b16 a[22:23], v1 offset:19840\n\t"
    "buffer_load_dwordx4 a[132:135], v0, s[44:47], s86 offen offset:2048 sc1\n\t"
    "v_and_b32 v23, s6, v37\n\t"
    "v_fma_f32 v22, v22, v14, v15\n\t"
    "v_mfma_f32_32x32x16_bf16 v[160:175], a[60:63], a[4:7], 0\n\t"
    "ds_read_b64_tr_b16 a[24:25], v1 offset:19392\n\t"
    "ds_read_b64_tr_b16 a[26:27], v1 offset:19904\n\t"
    "v_mul_f32 v80, v80, v112\n\t"
    "v_mul_f32 v81, v81, v113\n\t"
    "v_mul_f32 v82, v82, v114\n\t"
    "v_mul_f32 v83, v83, v115\n\t"
    "s_waitcnt lgkmcnt(6)\n\t"
    "v_mfma_f32_32x32x16_bf16 v[176:191], a[60:63], a[8:11], 0\n\t"
    "ds_read_b64_tr_b16 a[28:29], v1 offset:19200\n\t"
    "ds_read_b64_tr_b16 a[30:31], v1 offset:19712\n\t"
    "buffer_load_dwordx4 a[136:139], v0, s[44:47], s86 offen offset:3072 sc1\n\t"
    "v_mul_f32 v84, v84, v116\n\t"
    "v_mul_f32 v85, v85, v117\n\t"
    "v_mul_f32 v86, v86, v118\n\t"
    "v_mul_f32 v87, v87, v119\n\t"
    "v_mfma_f32_32x32x16_bf16 v[128:143], a[60:63], a[12:15], 0\n\t"
    "ds_read_b64_tr_b16 a[0:1], v1 offset:20288\n\t"
    "ds_read_b64_tr_b16 a[2:3], v1 offset:20800\n\t"
    "v_cvt_pk_bf16_f32 v72, v80, v81\n\t"
    "v_cvt_pk_bf16_f32 v73, v82, v83\n\t"
    "v_cvt_pk_bf16_f32 v74, v84, v85\n\t"
    "v_cvt_pk_bf16_f32 v75, v86, v87\n\t"
    "s_waitcnt lgkmcnt(6)\n\t"
    "v_mfma_f32_32x32x16_bf16 v[144:159], a[64:67], a[16:19], v[144:159]\n\t"
    "ds_read_b64_tr_b16 a[4:5], v1 offset:20352\n\t"
    "ds_read_b64_tr_b16 a[6:7], v1 offset:20864\n\t"
    "s_add_u32 s4, s55, 4096\n\t"
    "buffer_store_dwordx4 v[72:75], v30, s[44:47], s4 offen offset:2048 sc1\n\t"
    "v_mul_f32 v88, v88, v120\n\t"
    "v_mul_f32 v89, v89, v121\n\t"
    "v_mul_f32 v90, v90, v122\n\t"
    "v_mul_f32 v91, v91, v123\n\t"
    "v_mfma_f32_32x32x16_bf16 v[160:175], a[64:67], a[20:23], v[160:175]\n\t"
    "ds_read_b64_tr_b16 a[8:9], v1 offset:20416\n\t"
    "ds_read_b64_tr_b16 a[10:11], v1 offset:20928\n\t"
    "buffer_load_dwordx4 v12, s[36:39], s82 offen offset:2048 lds\n\t"
    "v_mul_f32 v92, v92, v124\n\t"
    "v_mul_f32 v93, v93, v125\n\t"
    "v_mul_f32 v94, v94, v126\n\t"
    "v_mul_f32 v95, v95, v127\n\t"
    "s_waitcnt lgkmcnt(6)\n\t"
    "v_mfma_f32_32x32x16_bf16 v[176:191], a[64:67], a[24:27], v[176:191]\n\t"
    "ds_read_b64_tr_b16 a[12:13], v1 offset:20224\n\t"
    "ds_read_b64_tr_b16 a[14:15], v1 offset:20736\n\t"
    "v_cvt_pk_bf16_f32 v76, v88, v89\n\t"
    "v_cvt_pk_bf16_f32 v77, v90, v91\n\t"
    "v_cvt_pk_bf16_f32 v78, v92, v93\n\t"
    "v_cvt_pk_bf16_f32 v79, v94, v95\n\t"
    "v_mfma_f32_32x32x16_bf16 v[128:143], a[64:67], a[28:31], v[128:143]\n\t"
    "ds_read_b64_tr_b16 a[16:17], v1 offset:21312\n\t"
    "ds_read_b64_tr_b16 a[18:19], v1 offset:21824\n\t"
    "s_add_u32 s4, s55, 4096\n\t"
    "buffer_store_dwordx4 v[76:79], v30, s[44:47], s4 offen offset:3072 sc1\n\t"
    "s_waitcnt lgkmcnt(6)\n\t"
    "v_mfma_f32_32x32x16_bf16 v[144:159], a[68:71], a[0:3], v[144:159]\n\t"
    "s_add_u32 m0, s52, 32768\n\t"
    "ds_read_b64_tr_b16 a[20:21], v1 offset:21376\n\t"
    "ds_read_b64_tr_b16 a[22:23], v1 offset:21888\n\t"
    "buffer_load_dwordx4 v12, s[36:39], s83 offen offset:3072 lds\n\t"
    "v_fma_f32 v23, v23, v14, v15\n\t"
    "v_mfma_f32_32x32x16_bf16 v[160:175], a[68:71], a[4:7], v[160:175]\n\t"
    "ds_read_b64_tr_b16 a[24:25], v1 offset:21440\n\t"
    "ds_read_b64_tr_b16 a[26:27], v1 offset:21952\n\t"
    "v_cvt_pk_bf16_f32 v45, v22, v23\n\t"
    "v_lshlrev_b32 v20, 16, v38\n\t"
    "s_waitcnt lgkmcnt(6)\n\t"
    "v_mfma_f32_32x32x16_bf16 v[176:191], a[68:71], a[8:11], v[176:191]\n\t"
    "s_add_u32 m0, s52, 35840\n\t"
    "ds_read_b64_tr_b16 a[28:29], v1 offset:21248\n\t"
    "ds_read_b64_tr_b16 a[30:31], v1 offset:21760\n\t"
    "buffer_load_dwordx4 v12, s[40:43], s81 offen offset:1024 lds\n\t"
    "v_and_b32 v21, s6, v38\n\t"
    "v_mfma_f32_32x32x16_bf16 v[128:143], a[68:71], a[12:15], v[128:143]\n\t"
    "s_waitcnt vmcnt(20)\n\t"
    "s_barrier\n\t"
    "ds_read_b64_tr_b16 a[0:1], v1 offset:22336\n\t"
    "ds_read_b64_tr_b16 a[2:3], v1 offset:22848\n\t"
    "s_waitcnt lgkmcnt(6)\n\t"
    "v_mfma_f32_32x32x16_bf16 v[144:159], a[72:75], a[16:19], v[144:159]\n\t"
    "ds_read_b64_tr_b16 a[4:5], v1 offset:22400\n\t"
    "ds_read_b64_tr_b16 a[6:7], v1 offset:22912\n\t"
    "s_mov_b32 s4, 1179648\n\t"
    "buffer_load_dwordx2 v[16:17], v19, s[44:47], s4 offen sc1\n\t"
    "v_fma_f32 v20, v20, v14, v15\n\t"
    "v_mfma_f32_32x32x16_bf16 v[160:175], a[72:75], a[20:23], v[160:175]\n\t"
    "ds_read_b64_tr_b16 a[8:9], v1 offset:22464\n\t"
    "ds_read_b64_tr_b16 a[10:11], v1 offset:22976\n\t"
    "buffer_load_dwordx4 v12, s[40:43], s82 offen offset:2048 lds\n\t"
    "v_fma_f32 v21, v21, v14, v15\n\t"
    "s_waitcnt lgkmcnt(6)\n\t"
    "v_mfma_f32_32x32x16_bf16 v[176:191], a[72:75], a[24:27], v[176:191]\n\t"
    "ds_read_b64_tr_b16 a[12:13], v1 offset:22272\n\t"
    "ds_read_b64_tr_b16 a[14:15], v1 offset:22784\n\t"
    "v_cvt_pk_bf16_f32 v46, v20, v21\n\t"
    "v_lshlrev_b32 v22, 16, v39\n\t"
    "v_and_b32 v23, s6, v39\n\t"
    "v_mfma_f32_32x32x16_bf16 v[128:143], a[72:75], a[28:31], v[128:143]\n\t"
    "s_sub_u32 m0, s52, 3072\n\t"
    "ds_read_b64_tr_b16 a[16:17], v1 offset:23360\n\t"
    "ds_read_b64_tr_b16 a[18:19], v1 offset:23872\n\t"
    "buffer_load_dwordx4 v12, s[40:43], s83 offen offset:3072 lds\n\t"
    "s_waitcnt lgkmcnt(6) vmcnt(22)\n\t"
    "v_mfma_f32_32x32x16_bf16 v[144:159], a[92:95], a[0:3], v[144:159]\n\t"
    "ds_read_b64_tr_b16 a[20:21], v1 offset:23424\n\t"
    "ds_read_b64_tr_b16 a[22:23], v1 offset:23936\n\t"
    "v_fma_f32 v22, v22, v14, v15\n\t"
    "v_fma_f32 v23, v23, v14, v15\n\t"
    "v_cvt_pk_bf16_f32 v47, v22, v23\n\t"
    "v_mfma_f32_32x32x16_bf16 v[160:175], a[92:95], a[4:7], v[160:175]\n\t"
    "s_add_u32 m0, s52, 1024\n\t"
    "ds_read_b64_tr_b16 a[24:25], v1 offset:23488\n\t"
    "ds_read_b64_tr_b16 a[26:27], v1 offset:24000\n\t"
    "buffer_load_dwordx4 v10, s[32:35], s77 offen lds\n\t"
    "s_waitcnt lgkmcnt(6)\n\t"
    "v_mfma_f32_32x32x16_bf16 v[176:191], a[92:95], a[8:11], v[176:191]\n\t"
    "ds_read_b64_tr_b16 a[28:29], v1 offset:23296\n\t"
    "ds_read_b64_tr_b16 a[30:31], v1 offset:23808\n\t"
    "v_mov_b32 v29, 0x10001\n\t"
    "v_bfe_u32 v252, v0, 4, 2\n\t"
    "v_mul_u32_u24 v252, 6, v252\n\t"
    "v_mfma_f32_32x32x16_bf16 v[128:143], a[92:95], a[12:15], v[128:143]\n\t"
    "s_add_u32 s5, s77, 15360\n\t"
    "ds_read_b64_tr_b16 a[0:1], v1 offset:24384\n\t"
    "ds_read_b64_tr_b16 a[2:3], v1 offset:24896\n\t"
    "buffer_load_dwordx4 v10, s[32:35], s5 offen offset:1024 lds\n\t"
    "s_waitcnt lgkmcnt(6) vmcnt(21)\n\t"
    "v_mfma_f32_32x32x16_bf16 v[144:159], a[96:99], a[16:19], v[144:159]\n\t"
    "ds_read_b64_tr_b16 a[4:5], v1 offset:24448\n\t"
    "ds_read_b64_tr_b16 a[6:7], v1 offset:24960\n\t"
    "v_add_u32 v252, 1, v252\n\t"
    "v_lshrrev_b32 v252, v252, s2\n\t"
    "v_lshlrev_b32 v252, 1, v252\n\t"
    "v_mfma_f32_32x32x16_bf16 v[160:175], a[96:99], a[20:23], v[160:175]\n\t"
    "s_add_u32 m0, s52, 3072\n\t"
    "ds_read_b64_tr_b16 a[8:9], v1 offset:24512\n\t"
    "ds_read_b64_tr_b16 a[10:11], v1 offset:25024\n\t"
    "buffer_load_dwordx4 v10, s[32:35], s78 offen lds\n\t"
    "s_waitcnt lgkmcnt(6)\n\t"
    "v_mfma_f32_32x32x16_bf16 v[176:191], a[96:99], a[24:27], v[176:191]\n\t"
    "ds_read_b64_tr_b16 a[12:13], v1 offset:24320\n\t"
    "ds_read_b64_tr_b16 a[14:15], v1 offset:24832\n\t"
    "v_and_b32 v252, 0x7e, v252\n\t"
    "s_and_b32 s97, s2, 1\n\t"
    "s_cselect_b32 s97, 1, 0x80\n\t"
    "v_or_b32 v252, s97, v252\n\t"
    "v_mfma_f32_32x32x16_bf16 v[128:143], a[96:99], a[28:31], v[128:143]\n\t"
    "s_add_u32 s5, s78, 15360\n\t"
    "ds_read_b64_tr_b16 a[16:17], v1 offset:25408\n\t"
    "ds_read_b64_tr_b16 a[18:19], v1 offset:25920\n\t"
    "buffer_load_dwordx4 v10, s[32:35], s5 offen offset:1024 lds\n\t"
    "s_waitcnt lgkmcnt(6) vmcnt(22)\n\t"
    "v_mfma_f32_32x32x16_bf16 v[144:159], a[100:103], a[0:3], v[144:159]\n\t"
    "ds_read_b64_tr_b16 a[20:21], v1 offset:25472\n\t"
    "ds_read_b64_tr_b16 a[22:23], v1 offset:25984\n\t"
    "v_bfe_u32 v248, v252, 0, 1\n\t"
    "v_bfe_u32 v253, v252, 1, 1\n\t"
    "v_lshl_or_b32 v248, v253, 16, v248\n\t"
    "v_mfma_f32_32x32x16_bf16 v[160:175], a[100:103], a[4:7], v[160:175]\n\t"
    "s_add_u32 s5, s77, 30720\n\t"
    "ds_read_b64_tr_b16 a[24:25], v1 offset:25536\n\t"
    "ds_read_b64_tr_b16 a[26:27], v1 offset:26048\n\t"
    "buffer_load_dwordx4 v10, s[32:35], s5 offen offset:2048 lds\n\t"
    "s_waitcnt lgkmcnt(6)\n\t"
    "v_mfma_f32_32x32x16_bf16 v[176:191], a[100:103], a[8:11], v[176:191]\n\t"
    "ds_read_b64_tr_b16 a[28:29], v1 offset:25344\n\t"
    "ds_read_b64_tr_b16 a[30:31], v1 offset:25856\n\t"
    "v_bfe_u32 v249, v252, 2, 1\n\t"
    "v_bfe_u32 v253, v252, 3, 1\n\t"
    "v_lshl_or_b32 v249, v253, 16, v249\n\t"
    "v_mfma_f32_32x32x16_bf16 v[128:143], a[100:103], a[12:15], v[128:143]\n\t"
    "s_add_u32 s5, s77, 46080\n\t"
    "ds_read_b64_tr_b16 a[0:1], v1 offset:26432\n\t"
    "ds_read_b64_tr_b16 a[2:3], v1 offset:26944\n\t"
    "buffer_load_dwordx4 v10, s[32:35], s5 offen offset:3072 lds\n\t"
    "s_waitcnt lgkmcnt(6) vmcnt(23)\n\t"
    "v_mfma_f32_32x32x16_bf16 v[144:159], a[104:107], a[16:19], v[144:159]\n\t"
    "ds_read_b64_tr_b16 a[4:5], v1 offset:26496\n\t"
    "ds_read_b64_tr_b16 a[6:7], v1 offset:27008\n\t"
    "v_bfe_u32 v250, v252, 4, 1\n\t"
    "v_bfe_u32 v253, v252, 5, 1\n\t"
    "v_lshl_or_b32 v250, v253, 16, v250\n\t"
    "v_mfma_f32_32x32x16_bf16 v[160:175], a[104:107], a[20:23], v[160:175]\n\t"
    "s_add_u32 m0, s52, 5120\n\t"
    "s_add_u32 s5, s78, 30720\n\t"
    "ds_read_b64_tr_b16 a[8:9], v1 offset:26560\n\t"
    "ds_read_b64_tr_b16 a[10:11], v1 offset:27072\n\t"
    "buffer_load_dwordx4 v10, s[32:35], s5 offen offset:2048 lds\n\t"
    "s_waitcnt lgkmcnt(6)\n\t"
    "v_mfma_f32_32x32x16_bf16 v[176:191], a[104:107], a[24:27], v[176:191]\n\t"
    "ds_read_b64_tr_b16 a[12:13], v1 offset:26368\n\t"
    "ds_read_b64_tr_b16 a[14:15], v1 offset:26880\n\t"
    "v_bfe_u32 v251, v252, 6, 1\n\t"
    "v_bfe_u32 v253, v252, 7, 1\n\t"
    "v_lshl_or_b32 v251, v253, 16, v251\n\t"
    "v_mfma_f32_32x32x16_bf16 v[128:143], a[104:107], a[28:31], v[128:143]\n\t"
    "s_add_u32 s5, s78, 46080\n\t"
    "ds_read_b64_tr_b16 a[16:17], v1 offset:28480\n\t"
    "ds_read_b64_tr_b16 a[18:19], v1 offset:28992\n\t"
    "buffer_load_dwordx4 v10, s[32:35], s5 offen offset:3072 lds\n\t"
    "s_waitcnt lgkmcnt(6) vmcnt(23)\n\t"
    "v_mfma_f32_32x32x16_bf16 v[144:159], a[108:111], a[0:3], v[144:159]\n\t"
    "ds_read_b64_tr_b16 a[20:21], v1 offset:28544\n\t"
    "ds_read_b64_tr_b16 a[22:23], v1 offset:29056\n\t"
    "v_mfma_f32_32x32x16_bf16 v[160:175], a[108:111], a[4:7], v[160:175]\n\t"
    "s_add_u32 m0, s52, 9216\n\t"
    "ds_read_b64_tr_b16 a[24:25], v1 offset:28608\n\t"
    "ds_read_b64_tr_b16 a[26:27], v1 offset:29120\n\t"
    "buffer_load_dwordx4 v10, s[32:35], s76 offen lds\n\t"
    "s_waitcnt lgkmcnt(6)\n\t"
    "v_mfma_f32_32x32x16_bf16 v[176:191], a[108:111], a[8:11], v[176:191]\n\t"
    "ds_read_b64_tr_b16 a[28:29], v1 offset:28416\n\t"
    "ds_read_b64_tr_b16 a[30:31], v1 offset:28928\n\t"
    "v_mfma_f32_32x32x16_bf16 v[128:143], a[108:111], a[12:15], v[128:143]\n\t"
    "s_add_u32 s5, s76, 15360\n\t"
    "ds_read_b64_tr_b16 a[0:1], v1 offset:30528\n\t"
    "ds_read_b64_tr_b16 a[2:3], v1 offset:31040\n\t"
    "buffer_load_dwordx4 v10, s[32:35], s5 offen offset:1024 lds\n\t"
    "s_waitcnt lgkmcnt(6) vmcnt(24)\n\t"
    "v_mfma_f32_32x32x16_bf16 v[144:159], a[112:115], a[16:19], v[144:159]\n\t"
    "ds_read_b64_tr_b16 a[4:5], v1 offset:30592\n\t"
    "ds_read_b64_tr_b16 a[6:7], v1 offset:31104\n\t"
    "v_mfma_f32_32x32x16_bf16 v[160:175], a[112:115], a[20:23], v[160:175]\n\t"
    "s_add_u32 s5, s76, 30720\n\t"
    "ds_read_b64_tr_b16 a[8:9], v1 offset:30656\n\t"
    "ds_read_b64_tr_b16 a[10:11], v1 offset:31168\n\t"
    "buffer_load_dwordx4 v10, s[32:35], s5 offen offset:2048 lds\n\t"
    "s_waitcnt lgkmcnt(6)\n\t"
    "v_mfma_f32_32x32x16_bf16 v[176:191], a[112:115], a[24:27], v[176:191]\n\t"
    "ds_read_b64_tr_b16 a[12:13], v1 offset:30464\n\t"
    "ds_read_b64_tr_b16 a[14:15], v1 offset:30976\n\t"
    "s_waitcnt vmcnt(16)\n\t"
    "v_mfma_f32_32x32x16_bf16 v[128:143], a[112:115], a[28:31], v[128:143]\n\t"
    "ds_read_b64_tr_b16 a[16:17], v1 offset:31552\n\t"
    "ds_read_b64_tr_b16 a[18:19], v1 offset:32064\n\t"
    "s_add_u32 s4, s87, 262144\n\t"
    "buffer_store_dwordx4 a[56:59], v18, s[44:47], s4 offen sc1\n\t"
    "s_waitcnt lgkmcnt(6)\n\t"
    "v_mfma_f32_32x32x16_bf16 v[144:159], a[116:119], a[0:3], v[144:159]\n\t"
    "ds_read_b64_tr_b16 a[20:21], v1 offset:31616\n\t"
    "ds_read_b64_tr_b16 a[22:23], v1 offset:32128\n\t"
    "s_waitcnt vmcnt(14)\n\t"
    "v_cmp_ne_u64 vcc, s[2:3], v[16:17]\n\t"
    "s_nop 3\n\t"
    "s_cbranch_vccz Lok2_%=\n\t"
    "s_mov_b32 s7, 0x4000\n\t"
    "Lsp2_%=:\n\t"
    "s_sleep 1\n\t"
    "s_mov_b32 s4, 1179648\n\t"
    "buffer_load_dwordx2 v[16:17], v19, s[44:47], s4 offen sc1\n\t"
    "s_waitcnt vmcnt(0)\n\t"
    "v_cmp_ne_u64 vcc, s[2:3], v[16:17]\n\t"
    "s_nop 4\n\t"
    "s_cbranch_vccz Lok2_%=\n\t"
    "s_sub_u32 s7, s7, 1\n\t"
    "s_cmp_lg_u32 s7, 0\n\t"
    "s_cbranch_scc1 Lsp2_%=\n\t"
    "Lok2_%=:\n\t"
    "s_add_u32 s4, s84, 8192\n\t"
    "buffer_load_dwordx4 a[140:143], v0, s[44:47], s4 offen sc1\n\t"
    "v_mfma_f32_32x32x16_bf16 v[160:175], a[116:119], a[4:7], v[160:175]\n\t"
    "ds_read_b64_tr_b16 a[24:25], v1 offset:31680\n\t"
    "ds_read_b64_tr_b16 a[26:27], v1 offset:32192\n\t"
    "s_waitcnt lgkmcnt(6)\n\t"
    "v_mfma_f32_32x32x16_bf16 v[176:191], a[116:119], a[8:11], v[176:191]\n\t"
    "ds_read_b64_tr_b16 a[28:29], v1 offset:31488\n\t"
    "ds_read_b64_tr_b16 a[30:31], v1 offset:32000\n\t"
    "s_add_u32 s4, s84, 8192\n\t"
    "buffer_load_dwordx4 a[144:147], v0, s[44:47], s4 offen offset:1024 sc1\n\t"
    "v_mfma_f32_32x32x16_bf16 v[128:143], a[116:119], a[12:15], v[128:143]\n\t"
    "ds_read_b64_tr_b16 a[0:1], v1 offset:27456\n\t"
    "ds_read_b64_tr_b16 a[2:3], v1 offset:27968\n\t"
    "s_waitcnt lgkmcnt(6)\n\t"
    "v_mfma_f32_32x32x16_bf16 v[144:159], a[120:123], a[16:19], v[144:159]\n\t"
    "ds_read_b64_tr_b16 a[4:5], v1 offset:27520\n\t"
    "ds_read_b64_tr_b16 a[6:7], v1 offset:28032\n\t"
    "s_add_u32 s4, s84, 8192\n\t"
    "buffer_load_dwordx4 a[148:151], v0, s[44:47], s4 offen offset:2048 sc1\n\t"
    "v_mfma_f32_32x32x16_bf16 v[160:175], a[120:123], a[20:23], v[160:175]\n\t"
    "ds_read_b64_tr_b16 a[8:9], v1 offset:27584\n\t"
    "ds_read_b64_tr_b16 a[10:11], v1 offset:28096\n\t"
    "s_waitcnt lgkmcnt(6)\n\t"
    "v_mfma_f32_32x32x16_bf16 v[176:191], a[120:123], a[24:27], v[176:191]\n\t"
    "ds_read_b64_tr_b16 a[12:13], v1 offset:29504\n\t"
    "ds_read_b64_tr_b16 a[14:15], v1 offset:30016\n\t"
    "s_add_u32 s4, s84, 8192\n\t"
    "buffer_load_dwordx4 a[152:155], v0, s[44:47], s4 offen offset:3072 sc1\n\t"
    "v_mfma_f32_32x32x16_bf16 v[128:143], a[120:123], a[28:31], v[128:143]\n\t"
    "ds_read_b64_tr_b16 a[16:17], v1 offset:29568\n\t"
    "ds_read_b64_tr_b16 a[18:19], v1 offset:30080\n\t"
    "s_waitcnt lgkmcnt(6)\n\t"
    "v_mfma_f32_32x32x16_bf16 v[144:159], a[124:127], a[0:3], v[144:159]\n\t"
    "ds_read_b64_tr_b16 a[20:21], v1 offset:29632\n\t"
    "ds_read_b64_tr_b16 a[22:23], v1 offset:30144\n\t"
    "s_add_u32 s4, s85, 8192\n\t"
    "buffer_load_dwordx4 a[156:159], v0, s[44:47], s4 offen sc1\n\t"
    "v_mfma_f32_32x32x16_bf16 v[160:175], a[124:127], a[4:7], v[160:175]\n\t"
    "ds_read_b64_tr_b16 a[24:25], v1 offset:32576\n\t"
    "ds_read_b64_tr_b16 a[26:27], v1 offset:33088\n\t"
    "s_waitcnt lgkmcnt(6)\n\t"
    "v_mfma_f32_32x32x16_bf16 v[176:191], a[124:127], a[8:11], v[176:191]\n\t"
    "ds_read_b64_tr_b16 a[28:29], v1 offset:32640\n\t"
    "ds_read_b64_tr_b16 a[30:31], v1 offset:33152\n\t"
    "s_add_u32 s4, s85, 8192\n\t"
    "buffer_load_dwordx4 a[160:163], v0, s[44:47], s4 offen offset:1024 sc1\n\t"
    "v_mfma_f32_32x32x16_bf16 v[144:159], a[128:131], a[12:15], v[144:159]\n\t"
    "ds_read_b64_tr_b16 a[0:1], v1 offset:32704\n\t"
    "ds_read_b64_tr_b16 a[2:3], v1 offset:33216\n\t"
    "s_waitcnt lgkmcnt(6)\n\t"
    "v_mfma_f32_32x32x16_bf16 v[160:175], a[128:131], a[16:19], v[160:175]\n\t"
    "ds_read_b64_tr_b16 a[4:5], v1 offset:33600\n\t"
    "ds_read_b64_tr_b16 a[6:7], v1 offset:34112\n\t"
    "s_add_u32 s4, s85, 8192\n\t"
    "buffer_load_dwordx4 a[164:167], v0, s[44:47], s4 offen offset:2048 sc1\n\t"
    "v_mfma_f32_32x32x16_bf16 v[176:191], a[128:131], a[20:23], v[176:191]\n\t"
    "ds_read_b64_tr_b16 a[8:9], v1 offset:33664\n\t"
    "ds_read_b64_tr_b16 a[10:11], v1 offset:34176\n\t"
    "s_waitcnt lgkmcnt(6)\n\t"
    "v_mfma_f32_32x32x16_bf16 v[144:159], a[132:135], a[24:27], v[144:159]\n\t"
    "ds_read_b64_tr_b16 a[12:13], v1 offset:33728\n\t"
    "ds_read_b64_tr_b16 a[14:15], v1 offset:34240\n\t"
    "s_add_u32 s4, s85, 8192\n\t"
    "buffer_load_dwordx4 a[168:171], v0, s[44:47], s4 offen offset:3072 sc1\n\t"
    "v_mfma_f32_32x32x16_bf16 v[160:175], a[132:135], a[28:31], v[160:175]\n\t"
    "ds_read_b128 a[16:19], v2 offset:40704\n\t"
    "s_waitcnt lgkmcnt(5)\n\t"
    "v_mfma_f32_32x32x16_bf16 v[176:191], a[132:135], a[0:3], v[176:191]\n\t"
    "ds_read_b128 a[20:23], v2 offset:41728\n\t"
    "s_add_u32 s4, s86, 8192\n\t"
    "buffer_load_dwordx4 a[172:175], v0, s[44:47], s4 offen sc1\n\t"
    "v_mfma_f32_32x32x16_bf16 v[144:159], a[136:139], a[4:7], v[144:159]\n\t"
    "ds_read_b128 a[24:27], v2 offset:42752\n\t"
    "s_waitcnt lgkmcnt(3)\n\t"
    "v_mfma_f32_32x32x16_bf16 v[160:175], a[136:139], a[8:11], v[160:175]\n\t"
    "ds_read_b128 a[28:31], v3 offset:40704\n\t"
    "s_add_u32 s4, s86, 8192\n\t"
    "buffer_load_dwordx4 a[176:179], v0, s[44:47], s4 offen offset:1024 sc1\n\t"
    "v_mfma_f32_32x32x16_bf16 v[176:191], a[136:139], a[12:15], v[176:191]\n\t"
    "ds_read_b128 a[0:3], v3 offset:41728\n\t"
    "s_waitcnt lgkmcnt(3)\n\t"
    "v_mfma_f32_32x32x16_bf16 v[144:159], a[76:79], a[16:19], v[144:159]\n\t"
    "ds_read_b128 a[4:7], v3 offset:42752\n\t"
    "s_add_u32 s4, s86, 8192\n\t"
    "buffer_load_dwordx4 a[180:183], v0, s[44:47], s4 offen offset:2048 sc1\n\t"
    "v_mfma_f32_32x32x16_bf16 v[160:175], a[76:79], a[20:23], v[160:175]\n\t"
    "ds_read_b128 a[8:11], v2 offset:37632\n\t"
    "s_waitcnt lgkmcnt(3)\n\t"
    "v_mfma_f32_32x32x16_bf16 v[176:191], a[76:79], a[24:27], v[176:191]\n\t"
    "ds_read_b128 a[12:15], v2 offset:38656\n\t"
    "s_add_u32 s4, s86, 8192\n\t"
    "buffer_load_dwordx4 a[184:187], v0, s[44:47], s4 offen offset:3072 sc1\n\t"
    "v_mfma_f32_32x32x16_bf16 v[144:159], a[80:83], a[28:31], v[144:159]\n\t"
    "ds_read_b128 a[16:19], v2 offset:39680\n\t"
    "s_waitcnt lgkmcnt(3)\n\t"
    "v_mfma_f32_32x32x16_bf16 v[160:175], a[80:83], a[0:3], v[160:175]\n\t"
    "s_add_u32 s5, s76, 46080\n\t"
    "ds_read_b128 a[20:23], v3 offset:37632\n\t"
    "buffer_load_dwordx4 v10, s[32:35], s5 offen offset:3072 lds\n\t"
    "v_mfma_f32_32x32x16_bf16 v[176:191], a[80:83], a[4:7], v[176:191]\n\t"
    "ds_read_b128 a[24:27], v3 offset:38656\n\t"
    "s_waitcnt lgkmcnt(3)\n\t"
    "v_mfma_f32_32x32x16_bf16 v[144:159], a[84:87], a[8:11], v[144:159]\n\t"
    "s_add_u32 m0, s52, 28672\n\t"
    "ds_read_b128 a[28:31], v3 offset:39680\n\t"
    "buffer_load_dwordx4 v11, s[36:39], s80 offen lds\n\t"
    "v_mfma_f32_32x32x16_bf16 v[160:175], a[84:87], a[12:15], v[160:175]\n\t"
    "s_waitcnt vmcnt(15)\n\t"
    "s_barrier\n\t"
    "ds_read_b128 a[0:3], v2 offset:40768\n\t"
    "s_waitcnt lgkmcnt(3)\n\t"
    "v_mfma_f32_32x32x16_bf16 v[176:191], a[84:87], a[16:19], v[176:191]\n\t"
    "ds_read_b128 a[4:7], v2 offset:41792\n\t"
    "s_mov_b32 s4, 1310720\n\t"
    "buffer_load_dwordx2 v[16:17], v19, s[44:47], s4 offen sc1\n\t"
    "v_mfma_f32_32x32x16_bf16 v[144:159], a[88:91], a[20:23], v[144:159]\n\t"
    "s_add_u32 m0, s52, 13312\n\t"
    "ds_read_b128 a[8:11], v2 offset:42816\n\t"
    "buffer_load_dwordx4 v11, s[40:43], s80 offen lds\n\t"
    "s_waitcnt lgkmcnt(3)\n\t"
    "v_mfma_f32_32x32x16_bf16 v[160:175], a[88:91], a[24:27], v[160:175]\n\t"
    "ds_read_b128 a[12:15], v3 offset:40768\n\t"
    "v_mfma_f32_32x32x16_bf16 v[176:191], a[88:91], a[28:31], v[176:191]\n\t"
    "s_add_u32 m0, s52, 14336\n\t"
    "ds_read_b128 a[16:19], v3 offset:41792\n\t"
    "buffer_load_dwordx4 v12, s[36:39], s80 offen lds\n\t"
    "s_waitcnt lgkmcnt(3) vmcnt(16)\n\t"
    "v_mfma_f32_32x32x16_bf16 v[144:159], a[140:143], a[0:3], v[144:159]\n\t"
    "ds_read_b128 a[20:23], v3 offset:42816\n\t"
    "v_mfma_f32_32x32x16_bf16 v[160:175], a[140:143], a[4:7], v[160:175]\n\t"
    "s_add_u32 m0, s52, 15360\n\t"
    "ds_read_b128 a[24:27], v2 offset:37696\n\t"
    "buffer_load_dwordx4 v12, s[40:43], s80 offen lds\n\t"
    "s_waitcnt lgkmcnt(3)\n\t"
    "v_mfma_f32_32x32x16_bf16 v[176:191], a[140:143], a[8:11], v[176:191]\n\t"
    "ds_read_b128 a[28:31], v2 offset:38720\n\t"
    "s_waitcnt vmcnt(16)\n\t"
    "v_mfma_f32_32x32x16_bf16 v[144:159], a[144:147], a[12:15], v[144:159]\n\t"
    "ds_read_b128 a[0:3], v2 offset:39744\n\t"
    "s_waitcnt lgkmcnt(3)\n\t"
    "v_mfma_f32_32x32x16_bf16 v[160:175], a[144:147], a[16:19], v[160:175]\n\t"
    "ds_read_b128 a[4:7], v3 offset:37696\n\t"
    "v_mfma_f32_32x32x16_bf16 v[176:191], a[144:147], a[20:23], v[176:191]\n\t"
    "ds_read_b128 a[8:11], v3 offset:38720\n\t"
    "s_waitcnt lgkmcnt(3) vmcnt(15)\n\t"
    "v_mfma_f32_32x32x16_bf16 v[144:159], a[148:151], a[24:27], v[144:159]\n\t"
    "ds_read_b128 a[12:15], v3 offset:39744\n\t"
    "v_mfma_f32_32x32x16_bf16 v[160:175], a[148:151], a[28:31], v[160:175]\n\t"
    "ds_read_b128 a[16:19], v2 offset:34560\n\t"
    "s_waitcnt lgkmcnt(3)\n\t"
    "v_mfma_f32_32x32x16_bf16 v[176:191], a[148:151], a[0:3], v[176:191]\n\t"
    "ds_read_b128 a[20:23], v2 offset:35584\n\t"
    "s_waitcnt vmcnt(14)\n\t"
    "v_mfma_f32_32x32x16_bf16 v[144:159], a[152:155], a[4:7], v[144:159]\n\t"
    "ds_read_b128 a[24:27], v2 offset:43776\n\t"
    "s_waitcnt lgkmcnt(3)\n\t"
    "v_mfma_f32_32x32x16_bf16 v[160:175], a[152:155], a[8:11], v[160:175]\n\t"
    "ds_read_b128 a[28:31], v3 offset:34560\n\t"
    "v_mfma_f32_32x32x16_bf16 v[176:191], a[152:155], a[12:15], v[176:191]\n\t"
    "ds_read_b128 a[0:3], v3 offset:35584\n\t"
    "s_waitcnt lgkmcnt(3) vmcnt(13)\n\t"
    "v_mfma_f32_32x32x16_bf16 v[144:159], a[156:159], a[16:19], v[144:159]\n\t"
    "ds_read_b128 a[4:7], v3 offset:43776\n\t"
    "v_mfma_f32_32x32x16_bf16 v[160:175], a[156:159], a[20:23], v[160:175]\n\t"
    "ds_read_b128 a[8:11], v2 offset:44800\n\t"
    "s_waitcnt lgkmcnt(3)\n\t"
    "v_mfma_f32_32x32x16_bf16 v[176:191], a[156:159], a[24:27], v[176:191]\n\t"
    "ds_read_b128 a[12:15], v2 offset:45824\n\t"
    "s_waitcnt vmcnt(12)\n\t"
    "v_mfma_f32_32x32x16_bf16 v[144:159], a[160:163], a[28:31], v[144:159]\n\t"
    "ds_read_b128 a[16:19], v2 offset:7936\n\t"
    "s_waitcnt lgkmcnt(3)\n\t"
    "v_mfma_f32_32x32x16_bf16 v[160:175], a[160:163], a[0:3], v[160:175]\n\t"
    "ds_read_b128 a[20:23], v3 offset:44800\n\t"
    "v_mfma_f32_32x32x16_bf16 v[176:191], a[160:163], a[4:7], v[176:191]\n\t"
    "ds_read_b128 a[24:27], v3 offset:45824\n\t"
    "s_waitcnt lgkmcnt(3) vmcnt(11)\n\t"
    "v_mfma_f32_32x32x16_bf16 v[144:159], a[164:167], a[8:11], v[144:159]\n\t"
    "ds_read_b128 a[28:31], v3 offset:7936\n\t"
    "v_mfma_f32_32x32x16_bf16 v[160:175], a[164:167], a[12:15], v[160:175]\n\t"
    "ds_read_b128 a[0:3], v2 offset:34624\n\t"
    "s_waitcnt lgkmcnt(3)\n\t"
    "v_mfma_f32_32x32x16_bf16 v[176:191], a[164:167], a[16:19], v[176:191]\n\t"
    "ds_read_b128 a[4:7], v2 offset:35648\n\t"
    "s_waitcnt vmcnt(3)\n\t"
    "v_cmp_ne_u64 vcc, s[2:3], v[16:17]\n\t"
    "s_nop 3\n\t"
    "s_cbranch_vccz Lok3_%=\n\t"
    "s_mov_b32 s7, 0x4000\n\t"
    "Lsp3_%=:\n\t"
    "s_sleep 1\n\t"
    "s_mov_b32 s4, 1310720\n\t"
    "buffer_load_dwordx2 v[16:17], v19, s[44:47], s4 offen sc1\n\t"
    "s_waitcnt vmcnt(0)\n\t"
    "v_cmp_ne_u64 vcc, s[2:3], v[16:17]\n\t"
    "s_nop 4\n\t"
    "s_cbranch_vccz Lok3_%=\n\t"
    "s_sub_u32 s7, s7, 1\n\t"
    "s_cmp_lg_u32 s7, 0\n\t"
    "s_cbranch_scc1 Lsp3_%=\n\t"
    "Lok3_%=:\n\t"
    "s_cmp_lg_u32 s94, 3\n\t"
    "s_cbranch_scc1 Lo34_%=\n\t"
    "buffer_load_dwordx4 v[64:67], v0, s[44:47], s28 offen sc1\n\t"
    "buffer_load_dwordx4 v[68:71], v0, s[44:47], s28 offen offset:1024 sc1\n\t"
    "buffer_load_dwordx4 v[72:75], v0, s[44:47], s28 offen offset:2048 sc1\n\t"
    "buffer_load_dwordx4 v[76:79], v0, s[44:47], s28 offen offset:3072 sc1\n\t"
    "Lo34_%=:\n\t"
    "buffer_load_dwordx4 a[188:191], v0, s[44:47], s29 offen sc1\n\t"
    "v_mfma_f32_32x32x16_bf16 v[144:159], a[168:171], a[20:23], v[144:159]\n\t"
    "ds_read_b128 a[8:11], v2 offset:43840\n\t"
    "s_waitcnt lgkmcnt(3)\n\t"
    "v_mfma_f32_32x32x16_bf16 v[160:175], a[168:171], a[24:27], v[160:175]\n\t"
    "ds_read_b128 a[12:15], v3 offset:34624\n\t"
    "buffer_load_dwordx4 a[192:195], v0, s[44:47], s29 offen offset:1024 sc1\n\t"
    "v_mfma_f32_32x32x16_bf16 v[176:191], a[168:171], a[28:31], v[176:191]\n\t"
    "ds_read_b128 a[16:19], v3 offset:35648\n\t"
    "s_waitcnt lgkmcnt(3)\n\t"
    "v_mfma_f32_32x32x16_bf16 v[144:159], a[172:175], a[0:3], v[144:159]\n\t"
    "ds_read_b128 a[20:23], v3 offset:43840\n\t"
    "buffer_load_dwordx4 a[204:207], v0, s[44:47], s30 offen sc1\n\t"
    "v_mfma_f32_32x32x16_bf16 v[160:175], a[172:175], a[4:7], v[160:175]\n\t"
    "ds_read_b128 a[24:27], v2 offset:44864\n\t"
    "s_waitcnt lgkmcnt(3)\n\t"
    "v_mfma_f32_32x32x16_bf16 v[176:191], a[172:175], a[8:11], v[176:191]\n\t"
    "ds_read_b128 a[28:31], v2 offset:45888\n\t"
    "Lpad5_%=:\n\t"
    "s_cbranch_execz Lpret_%=\n\t"
    "buffer_load_dwordx4 a[208:211], v0, s[44:47], s30 offen offset:1024 sc1\n\t"
    "v_mfma_f32_32x32x16_bf16 v[144:159], a[176:179], a[12:15], v[144:159]\n\t"
    "ds_read_b128 a[0:3], v2 offset:8000\n\t"
    "s_waitcnt lgkmcnt(3)\n\t"
    "v_mfma_f32_32x32x16_bf16 v[160:175], a[176:179], a[16:19], v[160:175]\n\t"
    "ds_read_b128 a[4:7], v3 offset:44864\n\t"
    "buffer_load_dwordx4 a[196:199], v0, s[44:47], s29 offen offset:2048 sc1\n\t"
    "v_mfma_f32_32x32x16_bf16 v[176:191], a[176:179], a[20:23], v[176:191]\n\t"
    "ds_read_b128 a[8:11], v3 offset:45888\n\t"
    "s_waitcnt lgkmcnt(3)\n\t"
    "v_mfma_f32_32x32x16_bf16 v[144:159], a[180:183], a[24:27], v[144:159]\n\t"
    "ds_read_b128 a[12:15], v3 offset:8000\n\t"
    "buffer_load_dwordx4 a[200:203], v0, s[44:47], s29 offen offset:3072 sc1\n\t"
    "v_mfma_f32_32x32x16_bf16 v[160:175], a[180:183], a[28:31], v[160:175]\n\t"
    "ds_read_b64_tr_b16 a[16:17], v1 offset:9024\n\t"
    "ds_read_b64_tr_b16 a[18:19], v1 offset:9536\n\t"
    "s_waitcnt lgkmcnt(4)\n\t"
    "v_mfma_f32_32x32x16_bf16 v[176:191], a[180:183], a[0:3], v[176:191]\n\t"
    "ds_read_b64_tr_b16 a[20:21], v1 offset:10048\n\t"
    "ds_read_b64_tr_b16 a[22:23], v1 offset:10560\n\t"
    "buffer_load_dwordx4 a[212:215], v0, s[44:47], s30 offen offset:2048 sc1\n\t"
    "v_mfma_f32_32x32x16_bf16 v[144:159], a[184:187], a[4:7], v[144:159]\n\t"
    "ds_read_b64_tr_b16 a[24:25], v1 offset:11072\n\t"
    "ds_read_b64_tr_b16 a[26:27], v1 offset:11584\n\t"
    "s_waitcnt lgkmcnt(6)\n\t"
    "v_mfma_f32_32x32x16_bf16 v[160:175], a[184:187], a[8:11], v[160:175]\n\t"
    "ds_read_b64_tr_b16 a[28:29], v1 offset:12096\n\t"
    "ds_read_b64_tr_b16 a[30:31], v1 offset:12608\n\t"
    "buffer_load_dwordx4 a[216:219], v0, s[44:47], s30 offen offset:3072 sc1\n\t"
    "v_mfma_f32_32x32x16_bf16 v[176:191], a[184:187], a[12:15], v[176:191]\n\t"
    "ds_read_b64_tr_b16 a[0:1], v1 offset:17216\n\t"
    "ds_read_b64_tr_b16 a[2:3], v1 offset:17728\n\t"
    "s_waitcnt lgkmcnt(6) vmcnt(7)\n\t"
    "v_mfma_f32_32x32x16_bf16 v[144:159], a[188:191], a[16:19], v[144:159]\n\t"
    "ds_read_b64_tr_b16 a[4:5], v1 offset:18240\n\t"
    "ds_read_b64_tr_b16 a[6:7], v1 offset:18752\n\t"
    "s_waitcnt vmcnt(6)\n\t"
    "v_mfma_f32_32x32x16_bf16 v[144:159], a[192:195], a[20:23], v[144:159]\n\t"
    "ds_read_b64_tr_b16 a[8:9], v1 offset:13120\n\t"
    "ds_read_b64_tr_b16 a[10:11], v1 offset:13632\n\t"
    "s_waitcnt lgkmcnt(6) vmcnt(5)\n\t"
    "v_mfma_f32_32x32x16_bf16 v[144:159], a[204:207], a[24:27], v[144:159]\n\t"
    "ds_read_b64_tr_b16 a[12:13], v1 offset:14144\n\t"
    "ds_read_b64_tr_b16 a[14:15], v1 offset:14656\n\t"
    "s_waitcnt vmcnt(4)\n\t"
    "v_mfma_f32_32x32x16_bf16 v[144:159], a[208:211], a[28:31], v[144:159]\n\t"
    "ds_read_b64_tr_b16 a[16:17], v1 offset:15168\n\t"
    "ds_read_b64_tr_b16 a[18:19], v1 offset:15680\n\t"
    "s_waitcnt lgkmcnt(6)\n\t"
    "v_mfma_f32_32x32x16_bf16 v[144:159], v[64:67], a[0:3], v[144:159]\n\t"
    "ds_read_b64_tr_b16 a[20:21], v1 offset:16192\n\t"
    "ds_read_b64_tr_b16 a[22:23], v1 offset:16704\n\t"
    "v_mfma_f32_32x32x16_bf16 v[144:159], v[68:71], a[4:7], v[144:159]\n\t"
    "ds_read_b64_tr_b16 a[24:25], v1 offset:19264\n\t"
    "ds_read_b64_tr_b16 a[26:27], v1 offset:19776\n\t"
    "s_waitcnt lgkmcnt(6) vmcnt(3)\n\t"
    "v_mfma_f32_32x32x16_bf16 v[144:159], a[196:199], a[8:11], v[144:159]\n\t"
    "s_barrier\n\t"
    "ds_read_b64_tr_b16 a[28:29], v1 offset:20288\n\t"
    "ds_read_b64_tr_b16 a[30:31], v1 offset:20800\n\t"
    "s_waitcnt vmcnt(2)\n\t"
    "v_mfma_f32_32x32x16_bf16 v[144:159], a[200:203], a[12:15], v[144:159]\n\t"
    "ds_read_b64_tr_b16 a[0:1], v1 offset:9088\n\t"
    "ds_read_b64_tr_b16 a[2:3], v1 offset:9600\n\t"
    "s_waitcnt lgkmcnt(6) vmcnt(1)\n\t"
    "v_mfma_f32_32x32x16_bf16 v[144:159], a[212:215], a[16:19], v[144:159]\n\t"
    "ds_read_b64_tr_b16 a[4:5], v1 offset:10112\n\t"
    "ds_read_b64_tr_b16 a[6:7], v1 offset:10624\n\t"
    "s_waitcnt vmcnt(0)\n\t"
    "v_mfma_f32_32x32x16_bf16 v[144:159], a[216:219], a[20:23], v[144:159]\n\t"
    "ds_read_b64_tr_b16 a[8:9], v1 offset:11136\n\t"
    "ds_read_b64_tr_b16 a[10:11], v1 offset:11648\n\t"
    "s_waitcnt lgkmcnt(6)\n\t"
    "v_mfma_f32_32x32x16_bf16 v[144:159], v[72:75], a[24:27], v[144:159]\n\t"
    "ds_read_b64_tr_b16 a[12:13], v1 offset:12160\n\t"
    "ds_read_b64_tr_b16 a[14:15], v1 offset:12672\n\t"
    "v_mfma_f32_32x32x16_bf16 v[144:159], v[76:79], a[28:31], v[144:159]\n\t"
    "ds_read_b64_tr_b16 a[16:17], v1 offset:17280\n\t"
    "ds_read_b64_tr_b16 a[18:19], v1 offset:17792\n\t"
    "s_waitcnt lgkmcnt(6)\n\t"
    "v_mfma_f32_32x32x16_bf16 v[160:175], a[188:191], a[0:3], v[160:175]\n\t"
    "ds_read_b64_tr_b16 a[20:21], v1 offset:18304\n\t"
    "ds_read_b64_tr_b16 a[22:23], v1 offset:18816\n\t"
    "v_mfma_f32_32x32x16_bf16 v[160:175], a[192:195], a[4:7], v[160:175]\n\t"
    "ds_read_b64_tr_b16 a[24:25], v1 offset:13184\n\t"
    "ds_read_b64_tr_b16 a[26:27], v1 offset:13696\n\t"
    "s_nop 2\n\t"
    "v_cvt_pk_bf16_f32 v144, v144, v145\n\t"
    "v_bfi_b32 v144, v29, v248, v144\n\t"
    "v_cvt_pk_bf16_f32 v145, v146, v147\n\t"
    "v_bfi_b32 v145, v29, v249, v145\n\t"
    "v_cvt_pk_bf16_f32 v146, v148, v149\n\t"
    "v_bfi_b32 v146, v29, v250, v146\n\t"
    "s_waitcnt lgkmcnt(6)\n\t"
    "v_mfma_f32_32x32x16_bf16 v[160:175], a[204:207], a[8:11], v[160:175]\n\t"
    "ds_read_b64_tr_b16 a[28:29], v1 offset:14208\n\t"
    "ds_read_b64_tr_b16 a[30:31], v1 offset:14720\n\t"
    "v_cvt_pk_bf16_f32 v147, v150, v151\n\t"
    "v_bfi_b32 v147, v29, v251, v147\n\t"
    "v_cvt_pk_bf16_f32 v148, v152, v153\n\t"
    "v_bfi_b32 v148, v29, v248, v148\n\t"
    "v_cvt_pk_bf16_f32 v149, v154, v155\n\t"
    "v_bfi_b32 v149, v29, v249, v149\n\t"
    "v_mfma_f32_32x32x16_bf16 v[160:175], a[208:211], a[12:15], v[160:175]\n\t"
    "ds_read_b64_tr_b16 a[0:1], v1 offset:15232\n\t"
    "ds_read_b64_tr_b16 a[2:3], v1 offset:15744\n\t"
    "buffer_store_dwordx4 v[144:147], v0, s[44:47], s88 offen sc1\n\t"
    "v_cvt_pk_bf16_f32 v150, v156, v157\n\t"
    "v_bfi_b32 v150, v29, v250, v150\n\t"
    "v_cvt_pk_bf16_f32 v151, v158, v159\n\t"
    "v_bfi_b32 v151, v29, v251, v151\n\t"
    "s_waitcnt lgkmcnt(6)\n\t"
    "v_mfma_f32_32x32x16_bf16 v[160:175], v[64:67], a[16:19], v[160:175]\n\t"
    "ds_read_b64_tr_b16 a[4:5], v1 offset:16256\n\t"
    "ds_read_b64_tr_b16 a[6:7], v1 offset:16768\n\t"
    "buffer_store_dwordx4 v[148:151], v0, s[44:47], s88 offen offset:1024 sc1\n\t"
    "v_mfma_f32_32x32x16_bf16 v[160:175], v[68:71], a[20:23], v[160:175]\n\t"
    "ds_read_b64_tr_b16 a[8:9], v1 offset:19328\n\t"
    "ds_read_b64_tr_b16 a[10:11], v1 offset:19840\n\t"
    "s_waitcnt lgkmcnt(6)\n\t"
    "v_mfma_f32_32x32x16_bf16 v[160:175], a[196:199], a[24:27], v[160:175]\n\t"
    "ds_read_b64_tr_b16 a[12:13], v1 offset:20352\n\t"
    "ds_read_b64_tr_b16 a[14:15], v1 offset:20864\n\t"
    "v_mfma_f32_32x32x16_bf16 v[160:175], a[200:203], a[28:31], v[160:175]\n\t"
    "ds_read_b64_tr_b16 a[16:17], v1 offset:9152\n\t"
    "ds_read_b64_tr_b16 a[18:19], v1 offset:9664\n\t"
    "s_waitcnt lgkmcnt(6)\n\t"
    "v_mfma_f32_32x32x16_bf16 v[160:175], a[212:215], a[0:3], v[160:175]\n\t"
    "ds_read_b64_tr_b16 a[20:21], v1 offset:10176\n\t"
    "ds_read_b64_tr_b16 a[22:23], v1 offset:10688\n\t"
    "v_mfma_f32_32x32x16_bf16 v[160:175], a[216:219], a[4:7], v[160:175]\n\t"
    "ds_read_b64_tr_b16 a[24:25], v1 offset:11200\n\t"
    "ds_read_b64_tr_b16 a[26:27], v1 offset:11712\n\t"
    "s_waitcnt lgkmcnt(6)\n\t"
    "v_mfma_f32_32x32x16_bf16 v[160:175], v[72:75], a[8:11], v[160:175]\n\t"
    "ds_read_b64_tr_b16 a[28:29], v1 offset:12224\n\t"
    "ds_read_b64_tr_b16 a[30:31], v1 offset:12736\n\t"
    "v_mfma_f32_32x32x16_bf16 v[160:175], v[76:79], a[12:15], v[160:175]\n\t"
    "ds_read_b64_tr_b16 a[0:1], v1 offset:17344\n\t"
    "ds_read_b64_tr_b16 a[2:3], v1 offset:17856\n\t"
    "s_waitcnt lgkmcnt(6)\n\t"
    "v_mfma_f32_32x32x16_bf16 v[176:191], a[188:191], a[16:19], v[176:191]\n\t"
    "ds_read_b64_tr_b16 a[4:5], v1 offset:18368\n\t"
    "ds_read_b64_tr_b16 a[6:7], v1 offset:18880\n\t"
    "v_mfma_f32_32x32x16_bf16 v[176:191], a[192:195], a[20:23], v[176:191]\n\t"
    "ds_read_b64_tr_b16 a[8:9], v1 offset:13248\n\t"
    "ds_read_b64_tr_b16 a[10:11], v1 offset:13760\n\t"
    "s_nop 2\n\t"
    "v_cvt_pk_bf16_f32 v160, v160, v161\n\t"
    "v_bfi_b32 v160, v29, v248, v160\n\t"
    "v_cvt_pk_bf16_f32 v161, v162, v163\n\t"
    "v_bfi_b32 v161, v29, v249, v161\n\t"
    "v_cvt_pk_bf16_f32 v162, v164, v165\n\t"
    "v_bfi_b32 v162, v29, v250, v162\n\t"
    "s_waitcnt lgkmcnt(6)\n\t"
    "v_mfma_f32_32x32x16_bf16 v[176:191], a[204:207], a[24:27], v[176:191]\n\t"
    "ds_read_b64_tr_b16 a[12:13], v1 offset:14272\n\t"
    "ds_read_b64_tr_b16 a[14:15], v1 offset:14784\n\t"
    "v_cvt_pk_bf16_f32 v163, v166, v167\n\t"
    "v_bfi_b32 v163, v29, v251, v163\n\t"
    "v_cvt_pk_bf16_f32 v164, v168, v169\n\t"
    "v_bfi_b32 v164, v29, v248, v164\n\t"
    "v_cvt_pk_bf16_f32 v165, v170, v171\n\t"
    "v_bfi_b32 v165, v29, v249, v165\n\t"
    "v_mfma_f32_32x32x16_bf16 v[176:191], a[208:211], a[28:31], v[176:191]\n\t"
    "ds_read_b64_tr_b16 a[16:17], v1 offset:15296\n\t"
    "ds_read_b64_tr_b16 a[18:19], v1 offset:15808\n\t"
    "buffer_store_dwordx4 v[160:163], v0, s[44:47], s89 offen sc1\n\t"
    "v_cvt_pk_bf16_f32 v166, v172, v173\n\t"
    "v_bfi_b32 v166, v29, v250, v166\n\t"
    "v_cvt_pk_bf16_f32 v167, v174, v175\n\t"
    "v_bfi_b32 v167, v29, v251, v167\n\t"
    "s_waitcnt lgkmcnt(6)\n\t"
    "v_mfma_f32_32x32x16_bf16 v[176:191], v[64:67], a[0:3], v[176:191]\n\t"
    "ds_read_b64_tr_b16 a[20:21], v1 offset:16320\n\t"
    "ds_read_b64_tr_b16 a[22:23], v1 offset:16832\n\t"
    "buffer_store_dwordx4 v[164:167], v0, s[44:47], s89 offen offset:1024 sc1\n\t"
    "v_mfma_f32_32x32x16_bf16 v[176:191], v[68:71], a[4:7], v[176:191]\n\t"
    "ds_read_b64_tr_b16 a[24:25], v1 offset:19392\n\t"
    "ds_read_b64_tr_b16 a[26:27], v1 offset:19904\n\t"
    "s_waitcnt lgkmcnt(6)\n\t"
    "v_mfma_f32_32x32x16_bf16 v[176:191], a[196:199], a[8:11], v[176:191]\n\t"
    "ds_read_b64_tr_b16 a[28:29], v1 offset:20416\n\t"
    "ds_read_b64_tr_b16 a[30:31], v1 offset:20928\n\t"
    "v_mfma_f32_32x32x16_bf16 v[176:191], a[200:203], a[12:15], v[176:191]\n\t"
    "ds_read_b64_tr_b16 a[0:1], v1 offset:27392\n\t"
    "ds_read_b64_tr_b16 a[2:3], v1 offset:27904\n\t"
    "s_waitcnt lgkmcnt(6)\n\t"
    "v_mfma_f32_32x32x16_bf16 v[176:191], a[212:215], a[16:19], v[176:191]\n\t"
    "ds_read_b64_tr_b16 a[4:5], v1 offset:29440\n\t"
    "ds_read_b64_tr_b16 a[6:7], v1 offset:29952\n\t"
    "v_mfma_f32_32x32x16_bf16 v[176:191], a[216:219], a[20:23], v[176:191]\n\t"
    "ds_read_b64_tr_b16 a[8:9], v1 offset:32512\n\t"
    "ds_read_b64_tr_b16 a[10:11], v1 offset:33024\n\t"
    "s_waitcnt lgkmcnt(6)\n\t"
    "v_mfma_f32_32x32x16_bf16 v[176:191], v[72:75], a[24:27], v[176:191]\n\t"
    "ds_read_b64_tr_b16 a[12:13], v1 offset:33536\n\t"
    "ds_read_b64_tr_b16 a[14:15], v1 offset:34048\n\t"
    "v_mfma_f32_32x32x16_bf16 v[176:191], v[76:79], a[28:31], v[176:191]\n\t"
    "ds_read_b128 a[16:19], v2 offset:36608\n\t"
    "s_waitcnt lgkmcnt(5)\n\t"
    "v_mfma_f32_32x32x16_bf16 v[128:143], a[124:127], a[0:3], v[128:143]\n\t"
    "ds_read_b128 a[20:23], v3 offset:36608\n\t"
    "v_mfma_f32_32x32x16_bf16 v[128:143], a[128:131], a[4:7], v[128:143]\n\t"
    "ds_read_b128 a[24:27], v2 offset:21248\n\t"
    "s_nop 5\n\t"
    "v_cvt_pk_bf16_f32 v176, v176, v177\n\t"
    "v_bfi_b32 v176, v29, v248, v176\n\t"
    "v_cvt_pk_bf16_f32 v177, v178, v179\n\t"
    "v_bfi_b32 v177, v29, v249, v177\n\t"
    "v_cvt_pk_bf16_f32 v178, v180, v181\n\t"
    "v_bfi_b32 v178, v29, v250, v178\n\t"
    "s_waitcnt lgkmcnt(3)\n\t"
    "v_mfma_f32_32x32x16_bf16 v[128:143], a[132:135], a[8:11], v[128:143]\n\t"
    "ds_read_b128 a[28:31], v3 offset:21248\n\t"
    "v_cvt_pk_bf16_f32 v179, v182, v183\n\t"
    "v_bfi_b32 v179, v29, v251, v179\n\t"
    "v_cvt_pk_bf16_f32 v180, v184, v185\n\t"
    "v_bfi_b32 v180, v29, v248, v180\n\t"
    "v_cvt_pk_bf16_f32 v181, v186, v187\n\t"
    "v_bfi_b32 v181, v29, v249, v181\n\t"
    "v_mfma_f32_32x32x16_bf16 v[128:143], a[136:139], a[12:15], v[128:143]\n\t"
    "ds_read_b128 a[0:3], v2 offset:36672\n\t"
    "buffer_store_dwordx4 v[176:179], v0, s[44:47], s90 offen sc1\n\t"
    "v_cvt_pk_bf16_f32 v182, v188, v189\n\t"
    "v_bfi_b32 v182, v29, v250, v182\n\t"
    "v_cvt_pk_bf16_f32 v183, v190, v191\n\t"
    "v_bfi_b32 v183, v29, v251, v183\n\t"
    "s_waitcnt lgkmcnt(3)\n\t"
    "v_mfma_f32_32x32x16_bf16 v[128:143], a[76:79], a[16:19], v[128:143]\n\t"
    "ds_read_b128 a[4:7], v3 offset:36672\n\t"
    "buffer_store_dwordx4 v[180:183], v0, s[44:47], s90 offen offset:1024 sc1\n\t"
    "v_mfma_f32_32x32x16_bf16 v[128:143], a[80:83], a[20:23], v[128:143]\n\t"
    "ds_read_b128 a[8:11], v2 offset:21312\n\t"
    "s_waitcnt lgkmcnt(3)\n\t"
    "v_mfma_f32_32x32x16_bf16 v[128:143], a[84:87], a[24:27], v[128:143]\n\t"
    "ds_read_b128 a[12:15], v3 offset:21312\n\t"
    "v_mfma_f32_32x32x16_bf16 v[128:143], a[88:91], a[28:31], v[128:143]\n\t"
    "ds_read_b128 a[16:19], v2 offset:22272\n\t"
    "s_waitcnt lgkmcnt(3)\n\t"
    "v_mfma_f32_32x32x16_bf16 v[128:143], a[140:143], a[0:3], v[128:143]\n\t"
    "ds_read_b128 a[20:23], v3 offset:22272\n\t"
    "v_mfma_f32_32x32x16_bf16 v[128:143], a[144:147], a[4:7], v[128:143]\n\t"
    "ds_read_b128 a[24:27], v2 offset:23296\n\t"
    "s_waitcnt lgkmcnt(3)\n\t"
    "v_mfma_f32_32x32x16_bf16 v[128:143], a[148:151], a[8:11], v[128:143]\n\t"
    "ds_read_b128 a[28:31], v3 offset:23296\n\t"
    "v_mfma_f32_32x32x16_bf16 v[128:143], a[152:155], a[12:15], v[128:143]\n\t"
    "ds_read_b128 a[0:3], v2 offset:22336\n\t"
    "s_waitcnt lgkmcnt(3)\n\t"
    "v_mfma_f32_32x32x16_bf16 v[128:143], a[156:159], a[16:19], v[128:143]\n\t"
    "ds_read_b128 a[4:7], v3 offset:22336\n\t"
    "v_mfma_f32_32x32x16_bf16 v[128:143], a[160:163], a[20:23], v[128:143]\n\t"
    "ds_read_b128 a[8:11], v2 offset:23360\n\t"
    "s_waitcnt lgkmcnt(3)\n\t"
    "v_mfma_f32_32x32x16_bf16 v[128:143], a[164:167], a[24:27], v[128:143]\n\t"
    "ds_read_b128 a[12:15], v3 offset:23360\n\t"
    "v_mfma_f32_32x32x16_bf16 v[128:143], a[168:171], a[28:31], v[128:143]\n\t"
    "ds_read_b64_tr_b16 a[16:17], v1 offset:8960\n\t"
    "ds_read_b64_tr_b16 a[18:19], v1 offset:9472\n\t"
    "s_waitcnt lgkmcnt(4)\n\t"
    "v_mfma_f32_32x32x16_bf16 v[128:143], a[172:175], a[0:3], v[128:143]\n\t"
    "ds_read_b64_tr_b16 a[20:21], v1 offset:9984\n\t"
    "ds_read_b64_tr_b16 a[22:23], v1 offset:10496\n\t"
    "v_mfma_f32_32x32x16_bf16 v[128:143], a[176:179], a[4:7], v[128:143]\n\t"
    "ds_read_b64_tr_b16 a[24:25], v1 offset:11008\n\t"
    "ds_read_b64_tr_b16 a[26:27], v1 offset:11520\n\t"
    "s_waitcnt lgkmcnt(6)\n\t"
    "v_mfma_f32_32x32x16_bf16 v[128:143], a[180:183], a[8:11], v[128:143]\n\t"
    "ds_read_b64_tr_b16 a[28:29], v1 offset:12032\n\t"
    "ds_read_b64_tr_b16 a[30:31], v1 offset:12544\n\t"
    "v_mfma_f32_32x32x16_bf16 v[128:143], a[184:187], a[12:15], v[128:143]\n\t"
    "ds_read_b64_tr_b16 a[0:1], v1 offset:17152\n\t"
    "ds_read_b64_tr_b16 a[2:3], v1 offset:17664\n\t"
    "s_waitcnt lgkmcnt(6)\n\t"
    "v_mfma_f32_32x32x16_bf16 v[128:143], a[188:191], a[16:19], v[128:143]\n\t"
    "ds_read_b64_tr_b16 a[4:5], v1 offset:18176\n\t"
    "ds_read_b64_tr_b16 a[6:7], v1 offset:18688\n\t"
    "v_mfma_f32_32x32x16_bf16 v[128:143], a[192:195], a[20:23], v[128:143]\n\t"
    "ds_read_b64_tr_b16 a[8:9], v1 offset:13056\n\t"
    "ds_read_b64_tr_b16 a[10:11], v1 offset:13568\n\t"
    "s_waitcnt lgkmcnt(6)\n\t"
    "v_mfma_f32_32x32x16_bf16 v[128:143], a[204:207], a[24:27], v[128:143]\n\t"
    "ds_read_b64_tr_b16 a[12:13], v1 offset:14080\n\t"
    "ds_read_b64_tr_b16 a[14:15], v1 offset:14592\n\t"
    "v_mfma_f32_32x32x16_bf16 v[128:143], a[208:211], a[28:31], v[128:143]\n\t"
    "ds_read_b64_tr_b16 a[16:17], v1 offset:15104\n\t"
    "ds_read_b64_tr_b16 a[18:19], v1 offset:15616\n\t"
    "s_waitcnt lgkmcnt(6)\n\t"
    "v_mfma_f32_32x32x16_bf16 v[128:143], v[64:67], a[0:3], v[128:143]\n\t"
    "ds_read_b64_tr_b16 a[20:21], v1 offset:16128\n\t"
    "ds_read_b64_tr_b16 a[22:23], v1 offset:16640\n\t"
    "v_mfma_f32_32x32x16_bf16 v[128:143], v[68:71], a[4:7], v[128:143]\n\t"
    "ds_read_b64_tr_b16 a[24:25], v1 offset:19200\n\t"
    "ds_read_b64_tr_b16 a[26:27], v1 offset:19712\n\t"
    "buffer_load_dwordx4 v[224:227], v0, s[44:47], s91 offen sc1\n\t"
    "s_waitcnt lgkmcnt(6)\n\t"
    "v_mfma_f32_32x32x16_bf16 v[128:143], a[196:199], a[8:11], v[128:143]\n\t"
    "ds_read_b64_tr_b16 a[28:29], v1 offset:20224\n\t"
    "ds_read_b64_tr_b16 a[30:31], v1 offset:20736\n\t"
    "buffer_load_dwordx4 v[228:231], v0, s[44:47], s91 offen offset:1024 sc1\n\t"
    "v_mfma_f32_32x32x16_bf16 v[128:143], a[200:203], a[12:15], v[128:143]\n\t"
    "s_waitcnt lgkmcnt(4)\n\t"
    "v_mfma_f32_32x32x16_bf16 v[128:143], a[212:215], a[16:19], v[128:143]\n\t"
    "v_mfma_f32_32x32x16_bf16 v[128:143], a[216:219], a[20:23], v[128:143]\n\t"
    "s_waitcnt lgkmcnt(0)\n\t"
    "v_mfma_f32_32x32x16_bf16 v[128:143], v[72:75], a[24:27], v[128:143]\n\t"
    "v_mfma_f32_32x32x16_bf16 v[128:143], v[76:79], a[28:31], v[128:143]\n\t"
    "v_mfma_f32_32x32x16_bf16 v[128:143], v[32:35], v[48:51], v[128:143]\n\t"
    "v_mfma_f32_32x32x16_bf16 v[128:143], v[40:43], v[56:59], v[128:143]\n\t"
    "v_mfma_f32_32x32x16_bf16 v[128:143], v[36:39], v[52:55], v[128:143]\n\t"
    "v_mfma_f32_32x32x16_bf16 v[128:143], v[44:47], v[60:63], v[128:143]\n\t"
    "s_sleep 3\n\t"
    "buffer_load_dwordx4 v[232:235], v0, s[44:47], s91 offen offset:2048 sc1\n\t"
    "buffer_load_dwordx4 v[236:239], v0, s[44:47], s91 offen offset:3072 sc1\n\t"
    "s_add_u32 s4, s91, 4096\n\t"
    "buffer_load_dwordx4 v[240:243], v0, s[44:47], s4 offen sc1\n\t"
    "s_add_u32 s4, s91, 4096\n\t"
    "buffer_load_dwordx4 v[244:247], v0, s[44:47], s4 offen offset:1024 sc1\n\t"
    "v_accvgpr_read_b32 v24, a50\n\t"
    "s_waitcnt vmcnt(5)\n\t"
    "v_xor_b32 v224, v248, v224\n\t"
    "v_xor_b32 v225, v249, v225\n\t"
    "v_xor_b32 v226, v250, v226\n\t"
    "v_xor_b32 v227, v251, v227\n\t"
    "s_waitcnt vmcnt(4)\n\t"
    "v_xor_b32 v228, v248, v228\n\t"
    "v_xor_b32 v229, v249, v229\n\t"
    "v_xor_b32 v230, v250, v230\n\t"
    "v_xor_b32 v231, v251, v231\n\t"
    "v_or3_b32 v20, v224, v225, v226\n\t"
    "v_or3_b32 v21, v227, v228, v229\n\t"
    "v_or3_b32 v20, v20, v230, v231\n\t"
    "v_or_b32 v20, v20, v21\n\t"
    "v_and_b32 v20, v29, v20\n\t"
    "v_cmp_ne_u32 vcc, 0, v20\n\t"
    "s_nop 3\n\t"
    "s_cbranch_vccz Lok11_%=\n\t"
    "s_mov_b32 s7, 0x4000\n\t"
    "Lsp11_%=:\n\t"
    "s_sleep 1\n\t"
    "buffer_load_dwordx4 v[224:227], v0, s[44:47], s91 offen sc1\n\t"
    "buffer_load_dwordx4 v[228:231], v0, s[44:47], s91 offen offset:1024 sc1\n\t"
    "s_waitcnt vmcnt(0)\n\t"
    "v_xor_b32 v224, v248, v224\n\t"
    "v_xor_b32 v225, v249, v225\n\t"
    "v_xor_b32 v226, v250, v226\n\t"
    "v_xor_b32 v227, v251, v227\n\t"
    "v_xor_b32 v228, v248, v228\n\t"
    "v_xor_b32 v229, v249, v229\n\t"
    "v_xor_b32 v230, v250, v230\n\t"
    "v_xor_b32 v231, v251, v231\n\t"
    "v_or3_b32 v20, v224, v225, v226\n\t"
    "v_or3_b32 v21, v227, v228, v229\n\t"
    "v_or3_b32 v20, v20, v230, v231\n\t"
    "v_or_b32 v20, v20, v21\n\t"
    "v_and_b32 v20, v29, v20\n\t"
    "v_cmp_ne_u32 vcc, 0, v20\n\t"
    "s_nop 4\n\t"
    "s_cbranch_vccz Lok11_%=\n\t"
    "s_sub_u32 s7, s7, 1\n\t"
    "s_cmp_lg_u32 s7, 0\n\t"
    "s_cbranch_scc1 Lsp11_%=\n\t"
    "Lok11_%=:\n\t"
    "v_dot2c_f32_bf16 v128, s53, v224\n\t"
    "v_dot2c_f32_bf16 v129, s54, v224\n\t"
    "v_dot2c_f32_bf16 v130, s53, v225\n\t"
    "v_dot2c_f32_bf16 v131, s54, v225\n\t"
    "v_dot2c_f32_bf16 v132, s53, v226\n\t"
    "v_dot2c_f32_bf16 v133, s54, v226\n\t"
    "v_dot2c_f32_bf16 v134, s53, v227\n\t"
    "v_dot2c_f32_bf16 v135, s54, v227\n\t"
    "v_dot2c_f32_bf16 v136, s53, v228\n\t"
    "v_dot2c_f32_bf16 v137, s54, v228\n\t"
    "v_dot2c_f32_bf16 v138, s53, v229\n\t"
    "v_dot2c_f32_bf16 v139, s54, v229\n\t"
    "v_dot2c_f32_bf16 v140, s53, v230\n\t"
    "v_dot2c_f32_bf16 v141, s54, v230\n\t"
    "v_dot2c_f32_bf16 v142, s53, v231\n\t"
    "v_dot2c_f32_bf16 v143, s54, v231\n\t"
    "s_waitcnt vmcnt(3)\n\t"
    "v_xor_b32 v232, v248, v232\n\t"
    "v_xor_b32 v233, v249, v233\n\t"
    "v_xor_b32 v234, v250, v234\n\t"
    "v_xor_b32 v235, v251, v235\n\t"
    "s_waitcnt vmcnt(2)\n\t"
    "v_xor_b32 v236, v248, v236\n\t"
    "v_xor_b32 v237, v249, v237\n\t"
    "v_xor_b32 v238, v250, v238\n\t"
    "v_xor_b32 v239, v251, v239\n\t"
    "v_or3_b32 v20, v232, v233, v234\n\t"
    "v_or3_b32 v21, v235, v236, v237\n\t"
    "v_or3_b32 v20, v20, v238, v239\n\t"
    "v_or_b32 v20, v20, v21\n\t"
    "v_and_b32 v20, v29, v20\n\t"
    "v_cmp_ne_u32 vcc, 0, v20\n\t"
    "s_nop 3\n\t"
    "s_cbranch_vccz Lok12_%=\n\t"
    "s_mov_b32 s7, 0x4000\n\t"
    "Lsp12_%=:\n\t"
    "s_sleep 1\n\t"
    "buffer_load_dwordx4 v[232:235], v0, s[44:47], s91 offen offset:2048 sc1\n\t"
    "buffer_load_dwordx4 v[236:239], v0, s[44:47], s91 offen offset:3072 sc1\n\t"
    "s_waitcnt vmcnt(0)\n\t"
    "v_xor_b32 v232, v248, v232\n\t"
    "v_xor_b32 v233, v249, v233\n\t"
    "v_xor_b32 v234, v250, v234\n\t"
    "v_xor_b32 v235, v251, v235\n\t"
    "v_xor_b32 v236, v248, v236\n\t"
    "v_xor_b32 v237, v249, v237\n\t"
    "v_xor_b32 v238, v250, v238\n\t"
    "v_xor_b32 v239, v251, v239\n\t"
    "v_or3_b32 v20, v232, v233, v234\n\t"
    "v_or3_b32 v21, v235, v236, v237\n\t"
    "v_or3_b32 v20, v20, v238, v239\n\t"
    "v_or_b32 v20, v20, v21\n\t"
    "v_and_b32 v20, v29, v20\n\t"
    "v_cmp_ne_u32 vcc, 0, v20\n\t"
    "s_nop 4\n\t"
    "s_cbranch_vccz Lok12_%=\n\t"
    "s_sub_u32 s7, s7, 1\n\t"
    "s_cmp_lg_u32 s7, 0\n\t"
    "s_cbranch_scc1 Lsp12_%=\n\t"
    "Lok12_%=:\n\t"
    "v_dot2c_f32_bf16 v128, s53, v232\n\t"
    "v_dot2c_f32_bf16 v129, s54, v232\n\t"
    "v_dot2c_f32_bf16 v130, s53, v233\n\t"
    "v_dot2c_f32_bf16 v131, s54, v233\n\t"
    "v_dot2c_f32_bf16 v132, s53, v234\n\t"
    "v_dot2c_f32_bf16 v133, s54, v234\n\t"
    "v_dot2c_f32_bf16 v134, s53, v235\n\t"
    "v_dot2c_f32_bf16 v135, s54, v235\n\t"
    "v_dot2c_f32_bf16 v136, s53, v236\n\t"
    "v_dot2c_f32_bf16 v137, s54, v236\n\t"
    "v_dot2c_f32_bf16 v138, s53, v237\n\t"
    "v_dot2c_f32_bf16 v139, s54, v237\n\t"
    "v_dot2c_f32_bf16 v140, s53, v238\n\t"
    "v_dot2c_f32_bf16 v141, s54, v238\n\t"
    "v_dot2c_f32_bf16 v142, s53, v239\n\t"
    "v_dot2c_f32_bf16 v143, s54, v239\n\t"
    "s_waitcnt vmcnt(1)\n\t"
    "v_xor_b32 v240, v248, v240\n\t"
    "v_xor_b32 v241, v249, v241\n\t"
    "v_xor_b32 v242, v250, v242\n\t"
    "v_xor_b32 v243, v251, v243\n\t"
    "s_waitcnt vmcnt(0)\n\t"
    "v_xor_b32 v244, v248, v244\n\t"
    "v_xor_b32 v245, v249, v245\n\t"
    "v_xor_b32 v246, v250, v246\n\t"
    "v_xor_b32 v247, v251, v247\n\t"
    "v_or3_b32 v20, v240, v241, v242\n\t"
    "v_or3_b32 v21, v243, v244, v245\n\t"
    "v_or3_b32 v20, v20, v246, v247\n\t"
    "v_or_b32 v20, v20, v21\n\t"
    "v_and_b32 v20, v29, v20\n\t"
    "v_cmp_ne_u32 vcc, 0, v20\n\t"
    "s_nop 3\n\t"
    "s_cbranch_vccz Lok13_%=\n\t"
    "s_mov_b32 s7, 0x4000\n\t"
    "Lsp13_%=:\n\t"
    "s_sleep 1\n\t"
    "s_add_u32 s4, s91, 4096\n\t"
    "buffer_load_dwordx4 v[240:243], v0, s[44:47], s4 offen sc1\n\t"
    "s_add_u32 s4, s91, 4096\n\t"
    "buffer_load_dwordx4 v[244:247], v0, s[44:47], s4 offen offset:1024 sc1\n\t"
    "s_waitcnt vmcnt(0)\n\t"
    "v_xor_b32 v240, v248, v240\n\t"
    "v_xor_b32 v241, v249, v241\n\t"
    "v_xor_b32 v242, v250, v242\n\t"
    "v_xor_b32 v243, v251, v243\n\t"
    "v_xor_b32 v244, v248, v244\n\t"
    "v_xor_b32 v245, v249, v245\n\t"
    "v_xor_b32 v246, v250, v246\n\t"
    "v_xor_b32 v247, v251, v247\n\t"
    "v_or3_b32 v20, v240, v241, v242\n\t"
    "v_or3_b32 v21, v243, v244, v245\n\t"
    "v_or3_b32 v20, v20, v246, v247\n\t"
    "v_or_b32 v20, v20, v21\n\t"
    "v_and_b32 v20, v29, v20\n\t"
    "v_cmp_ne_u32 vcc, 0, v20\n\t"
    "s_nop 4\n\t"
    "s_cbranch_vccz Lok13_%=\n\t"
    "s_sub_u32 s7, s7, 1\n\t"
    "s_cmp_lg_u32 s7, 0\n\t"
    "s_cbranch_scc1 Lsp13_%=\n\t"
    "Lok13_%=:\n\t"
    "v_dot2c_f32_bf16 v128, s53, v240\n\t"
    "v_dot2c_f32_bf16 v129, s54, v240\n\t"
    "v_dot2c_f32_bf16 v130, s53, v241\n\t"
    "v_dot2c_f32_bf16 v131, s54, v241\n\t"
    "v_dot2c_f32_bf16 v132, s53, v242\n\t"
    "v_dot2c_f32_bf16 v133, s54, v242\n\t"
    "s_mov_b32 s4, 0\n\t"
    "buffer_store_dword v128, v24, s[48:51], s4 offen\n\t"
    "buffer_store_dword v129, v24, s[48:51], s4 offen offset:2048\n\t"
    "v_dot2c_f32_bf16 v134, s53, v243\n\t"
    "v_dot2c_f32_bf16 v135, s54, v243\n\t"
    "s_mov_b32 s4, 4096\n\t"
    "buffer_store_dword v130, v24, s[48:51], s4 offen\n\t"
    "buffer_store_dword v131, v24, s[48:51], s4 offen offset:2048\n\t"
    "v_dot2c_f32_bf16 v136, s53, v244\n\t"
    "v_dot2c_f32_bf16 v137, s54, v244\n\t"
    "s_mov_b32 s4, 8192\n\t"
    "buffer_store_dword v132, v24, s[48:51], s4 offen\n\t"
    "buffer_store_dword v133, v24, s[48:51], s4 offen offset:2048\n\t"
    "v_dot2c_f32_bf16 v138, s53, v245\n\t"
    "v_dot2c_f32_bf16 v139, s54, v245\n\t"
    "s_mov_b32 s4, 12288\n\t"
    "buffer_store_dword v134, v24, s[48:51], s4 offen\n\t"
    "buffer_store_dword v135, v24, s[48:51], s4 offen offset:2048\n\t"
    "v_dot2c_f32_bf16 v140, s53, v246\n\t"
    "v_dot2c_f32_bf16 v141, s54, v246\n\t"
    "s_mov_b32 s4, 16384\n\t"
    "buffer_store_dword v136, v24, s[48:51], s4 offen\n\t"
    "buffer_store_dword v137, v24, s[48:51], s4 offen offset:2048\n\t"
    "v_dot2c_f32_bf16 v142, s53, v247\n\t"
    "v_dot2c_f32_bf16 v143, s54, v247\n\t"
    "s_mov_b32 s4, 20480\n\t"
    "buffer_store_dword v138, v24, s[48:51], s4 offen\n\t"
    "buffer_store_dword v139, v24, s[48:51], s4 offen offset:2048\n\t"
    "s_mov_b32 s4, 24576\n\t"
    "buffer_store_dword v140, v24, s[48:51], s4 offen\n\t"
    "buffer_store_dword v141, v24, s[48:51], s4 offen offset:2048\n\t"
    "s_mov_b32 s4, 28672\n\t"
    "buffer_store_dword v142, v24, s[48:51], s4 offen\n\t"
    "buffer_store_dword v143, v24, s[48:51], s4 offen offset:2048\n\t"
    "s_cmp_lg_u32 s96, 960\n\t"
    "s_cbranch_scc1 Lnoe_%=\n\t"
    "s_mov_b32 s4, 655360\n\t"
    "buffer_store_dword a56, v18, s[44:47], s4 offen sc1\n\t"
    "Lnoe_%=:\n\t"
    "Lend_%=:\n\t"
      : "+{v0}"(tid), "+{s2}"(bid), "+{s[0:1]}"(karg)
      : "s"((u32)(uintptr_t)smem)
      : "memory", "scc", "vcc", "s99", "v255", "a255");
}

extern "C" void kernel_launch(void* const* d_in, const int* in_sizes, int n_in, void* d_out, int out_size, void* d_ws, size_t ws_size,
                              hipStream_t stream) {
  const u32 F = 0x20000u;
  s5_glu_fused<<<256, 256, 0, stream>>>(d_in[0], 2048u * 1024u, F, d_in[3], 512u * 1024u, F, d_in[4], 512u * 1024u + 1024u, F,
                                        d_in[10], 512u * 3584u, F, d_in[11], 512u * 3584u + 2048u, F, d_in[10], 512u * 3584u, F,
                                        d_in[12], 1792u * 1024u, F, d_in[5], 512u * 1024u + 3072u, F, d_in[6], 512u * 1024u + 3072u, F,
                                        d_ws, 20971520u, F, d_out, 2048u * 2048u, F, d_in[1], d_in[2], d_in[7], d_in[8]);
}
```
